```python
import torch

def _softmax(x, axis=-1):
    return torch.softmax(x, dim=axis)

def _gelu(x):
    return torch.nn.functional.gelu(x, approximate='tanh')

def _silu(x):
    return torch.nn.functional.silu(x)

def _sigmoid(x):
    return torch.sigmoid(x)

def _layernorm(x, axis=-1, eps=1e-05):
    m = x.mean(dim=axis, keepdim=True)
    v = x.var(dim=axis, keepdim=True, unbiased=False)
    return (x - m) / torch.sqrt(v + eps)

def _scatter_add(tgt, idx, src, axis=0):
    return tgt.clone().index_add_(axis, idx, src)

def _segment_sum(src, seg, n):
    out = torch.zeros((n,) + src.shape[1:], dtype=src.dtype)
    return out.index_add_(0, seg, src)

def _pad(x, pw):
    flat = tuple((v for pair in reversed(pw) for v in pair))
    return torch.nn.functional.pad(x, flat)
kernel_name = 'adaLN_cond__gqa_block_cached__swiglu_ffn__decode'

def setup_inputs():

    _w = lambda a: (lambda t: t.to(torch.bfloat16) if t.is_floating_point() else t)(torch.as_tensor(a))

    def _A_setup():
        B, Sq, D = (1, 1, 512)
        Hq, Hkv, Sk, Dh = (32, 8, 65536, 128)
        assert Hq % Hkv == 0, (Hq, Hkv)
        return {'x': _w((lambda *s: torch.randn(*s, dtype=torch.bfloat16) * 0.05)(B, Sq, D)), 'k_cache': _w((lambda *s: torch.randn(*s, dtype=torch.bfloat16) * 3.2)(B, Hkv, Sk, Dh)), 'v_cache': _w((lambda *s: torch.randn(*s, dtype=torch.bfloat16) * 0.4)(B, Hkv, Sk, Dh)), 'wq': _w((lambda *s: torch.randn(*s, dtype=torch.bfloat16) * 3.2)(D, Hq * Dh) * D ** (-0.5)), 'wo': _w((lambda *s: torch.randn(*s, dtype=torch.bfloat16) * 0.4)(Hq * Dh, D) * (Hq * Dh) ** (-0.5))}
    _A_in = _A_setup()

    def _B_setup():
        B, S, D, F = (1, 1, 512, 1792)
        return {'x': _w((lambda *s: torch.randn(*s, dtype=torch.bfloat16) * 0.05)(B, S, D)), 'w_gate': _w((lambda *s: torch.randn(*s, dtype=torch.bfloat16) * 0.05)(D, F)), 'w_up': _w((lambda *s: torch.randn(*s, dtype=torch.bfloat16) * 0.05)(D, F)), 'w_down': _w((lambda *s: torch.randn(*s, dtype=torch.bfloat16) * 0.05)(F, D))}
    _B_in = _B_setup()
    return {'A_x': _A_in['x'], 'A_k_cache': _A_in['k_cache'], 'A_v_cache': _A_in['v_cache'], 'A_wq': _A_in['wq'], 'A_wo': _A_in['wo'], 'B_x': _B_in['x'], 'B_w_gate': _B_in['w_gate'], 'B_w_up': _B_in['w_up'], 'B_w_down': _B_in['w_down'], 'c': _w((lambda *s: torch.randn(*s, dtype=torch.bfloat16) * 0.2)(1, 512) * 8.0), 'w_mod': _w((lambda *s: torch.randn(*s, dtype=torch.bfloat16) * 0.2)(512, 6 * 512) * 512 ** (-0.5) * 8.0)}

def reference(A_x, A_k_cache, A_v_cache, A_wq, A_wo, B_x, B_w_gate, B_w_up, B_w_down, c, w_mod):

    def _A_ref(x, k_cache, v_cache, wq, wo):
        B, Sq, D = x.shape
        Hq, Dh = (32, 128)
        Hkv = k_cache.shape[1]
        rep = Hq // Hkv
        q = (lambda x, *d: x.permute(*d))((x @ wq).reshape(B, Sq, Hq, Dh), 0, 2, 1, 3)
        k = (lambda x, n, axis: torch.repeat_interleave(x, n, dim=axis))(k_cache, rep, axis=1)
        v = (lambda x, n, axis: torch.repeat_interleave(x, n, dim=axis))(v_cache, rep, axis=1)
        scores = torch.einsum('bhqd,bhkd->bhqk', q, k) * Dh ** (-0.5)
        p = _softmax(scores, axis=-1)
        o = torch.einsum('bhqk,bhkd->bhqd', p, v)
        return (lambda x, *d: x.permute(*d))(o, 0, 2, 1, 3).reshape(B, Sq, Hq * Dh) @ wo

    def _B_ref(x, w_gate, w_up, w_down):
        g = _silu(x @ w_gate)
        u = x @ w_up
        return g * u @ w_down
    x = A_x
    mod = (_silu(c) @ w_mod).reshape(c.shape[0], 6, 1, -1)
    sa, ca, ga, sb, cb, gb = (mod[:, 0].clone(), mod[:, 1].clone(), mod[:, 2].clone(), mod[:, 3].clone(), mod[:, 4].clone(), mod[:, 5].clone())
    h = _layernorm(x, axis=-1) * (1 + ca) + sa
    x = x + ga * _A_ref(h, A_k_cache, A_v_cache, A_wq, A_wo)
    h = _layernorm(x, axis=-1) * (1 + cb) + sb
    x = x + gb * _B_ref(h, B_w_gate, B_w_up, B_w_down)
    return x
```

```cpp
#include <hip/hip_runtime.h>
#include <hip/hip_bf16.h>
#include <stdint.h>

typedef unsigned short u16;
typedef unsigned u32;
typedef u32 u32x4 __attribute__((ext_vector_type(4)));
typedef float f32x4 __attribute__((ext_vector_type(4)));

constexpr int D = 512, HQ = 32, HKV = 8, SK = 65536, DH = 128, F = 1792;
constexpr int NSPLIT = 64;

__device__ __forceinline__ float bf2f(u16 h) { return __uint_as_float(((u32)h) << 16); }
__device__ __forceinline__ u16 f2bf(float f) { __bf16 b = (__bf16)f; return __builtin_bit_cast(u16, b); }
__device__ __forceinline__ float rbf(float f) { return bf2f(f2bf(f)); }
__device__ __forceinline__ float silu(float x) { return x / (1.f + __expf(-x)); }

__device__ float gemv_col(const float* in, const u16* w, int K, int N, int n) {
  float a = 0.f;
  for (int k = 0; k < K; ++k) a = fmaf(in[k], bf2f(w[(size_t)k * N + n]), a);
  return a;
}

__global__ void k_mod(const u16* c, const u16* wmod, float* mod) {
  __shared__ float s[D];
  for (int i = threadIdx.x; i < D; i += blockDim.x) s[i] = rbf(silu(bf2f(c[i])));
  __syncthreads();
  int n = blockIdx.x * blockDim.x + threadIdx.x;
  mod[n] = rbf(gemv_col(s, wmod, D, 6 * D, n));
}

__device__ void adaln(const float* x, const float* sh, const float* sc, float* h) {
  float sum = 0.f;
  for (int i = 0; i < D; ++i) sum += x[i];
  float mean = sum / D;
  float var = 0.f;
  for (int i = 0; i < D; ++i) { float d = x[i] - mean; var += d * d; }
  var /= D;
  float m = rbf(mean), v = rbf(var);
  float den = rbf(sqrtf(rbf(v + 1e-5f)));
  for (int i = threadIdx.x; i < D; i += blockDim.x) {
    float ln = rbf(rbf(x[i] - m) / den);
    h[i] = rbf(rbf(ln * rbf(1.f + sc[i])) + sh[i]);
  }
}

__global__ void k_q(const u16* x, const float* mod, const u16* wq, u16* q) {
  __shared__ float sx[D], h[D];
  for (int i = threadIdx.x; i < D; i += blockDim.x) sx[i] = bf2f(x[i]);
  __syncthreads();
  adaln(sx, mod, mod + D, h);
  __syncthreads();
  int n = blockIdx.x * blockDim.x + threadIdx.x;
  q[n] = f2bf(gemv_col(h, wq, D, HQ * DH, n));
}

__global__ void __launch_bounds__(256) k_attn(const u16* q, const u16* kc, const u16* vc, float* part) {
  constexpr int KS = SK / NSPLIT;
  __shared__ float sq[4][DH];
  __shared__ float sc[4][KS];
  __shared__ float red[4][256];
  __shared__ float sm[4], sl[4];
  int s = blockIdx.x, hk = blockIdx.y, t = threadIdx.x;
  for (int i = t; i < 4 * DH; i += 256) sq[i / DH][i % DH] = bf2f(q[hk * 4 * DH + i]);
  __syncthreads();
  const u16* kb = kc + ((size_t)hk * SK + (size_t)s * KS) * DH;
  const u16* vb = vc + ((size_t)hk * SK + (size_t)s * KS) * DH;
  float mx[4] = {-INFINITY, -INFINITY, -INFINITY, -INFINITY};
  for (int j = t; j < KS; j += 256) {
    float a[4] = {0, 0, 0, 0};
    _Pragma("unroll 1") for (int d8 = 0; d8 < DH / 8; ++d8) {
      u32x4 v = *(const u32x4*)(kb + (size_t)j * DH + d8 * 8);
      for (int e = 0; e < 4; ++e) {
        float lo = __uint_as_float(v[e] << 16), hi = __uint_as_float(v[e] & 0xffff0000u);
        for (int g = 0; g < 4; ++g) a[g] = fmaf(hi, sq[g][d8 * 8 + 2 * e + 1], fmaf(lo, sq[g][d8 * 8 + 2 * e], a[g]));
      }
    }
    for (int g = 0; g < 4; ++g) { float z = rbf(rbf(a[g]) * 0.08838834764831845f); sc[g][j] = z; mx[g] = fmaxf(mx[g], z); }
  }
  for (int g = 0; g < 4; ++g) red[g][t] = mx[g];
  __syncthreads();
  if (t < 4) { float m = -INFINITY; for (int i = 0; i < 256; ++i) m = fmaxf(m, red[t][i]); sm[t] = m; }
  __syncthreads();
  float ls[4] = {0, 0, 0, 0};
  for (int j = t; j < KS; j += 256) for (int g = 0; g < 4; ++g) { float p = __expf(sc[g][j] - sm[g]); ls[g] += p; sc[g][j] = p; }
  for (int g = 0; g < 4; ++g) red[g][t] = ls[g];
  __syncthreads();
  if (t < 4) { float m = 0; for (int i = 0; i < 256; ++i) m += red[t][i]; sl[t] = m; }
  __syncthreads();
  int d = t & 127, hf = t >> 7;
  float o[4] = {0, 0, 0, 0};
  for (int j = hf; j < KS; j += 2) { float v = bf2f(vb[(size_t)j * DH + d]); for (int g = 0; g < 4; ++g) o[g] = fmaf(sc[g][j], v, o[g]); }
  for (int g = 0; g < 4; ++g) red[g][t] = o[g];
  __syncthreads();
  if (t < 128) for (int g = 0; g < 4; ++g) part[((size_t)(hk * 4 + g) * NSPLIT + s) * 132 + 4 + t] = red[g][t] + red[g][t + 128];
  if (t < 4) { part[((size_t)(hk * 4 + t) * NSPLIT + s) * 132] = sm[t]; part[((size_t)(hk * 4 + t) * NSPLIT + s) * 132 + 1] = sl[t]; }
}

__global__ void k_comb(const float* part, float* o) {
  int h = blockIdx.x, d = threadIdx.x;
  float M = -INFINITY;
  for (int s = 0; s < NSPLIT; ++s) M = fmaxf(M, part[((size_t)h * NSPLIT + s) * 132]);
  float L = 0, a = 0;
  for (int s = 0; s < NSPLIT; ++s) { const float* p = part + ((size_t)h * NSPLIT + s) * 132; float w = __expf(p[0] - M); L += w * p[1]; a += w * p[4 + d]; }
  o[h * DH + d] = rbf(a / L);
}

__global__ void k_wo(const float* o, const u16* wo, const u16* x, const float* mod, float* x1) {
  __shared__ float so[HQ * DH];
  for (int i = threadIdx.x; i < HQ * DH; i += blockDim.x) so[i] = o[i];
  __syncthreads();
  int n = blockIdx.x * blockDim.x + threadIdx.x;
  float a = rbf(gemv_col(so, wo, HQ * DH, D, n));
  x1[n] = rbf(bf2f(x[n]) + rbf(mod[2 * D + n] * a));
}

__global__ void k_ffn1(const float* x1, const float* mod, const u16* wg, const u16* wu, float* act) {
  __shared__ float sx[D], h[D];
  for (int i = threadIdx.x; i < D; i += blockDim.x) sx[i] = x1[i];
  __syncthreads();
  adaln(sx, mod + 3 * D, mod + 4 * D, h);
  __syncthreads();
  int n = blockIdx.x * blockDim.x + threadIdx.x;
  float g = rbf(silu(rbf(gemv_col(h, wg, D, F, n))));
  float u = rbf(gemv_col(h, wu, D, F, n));
  act[n] = rbf(g * u);
}

__global__ void k_ffn2(const float* act, const u16* wd, const float* x1, const float* mod, u16* out) {
  __shared__ float sa[F];
  for (int i = threadIdx.x; i < F; i += blockDim.x) sa[i] = act[i];
  __syncthreads();
  int n = blockIdx.x * blockDim.x + threadIdx.x;
  float a = rbf(gemv_col(sa, wd, F, D, n));
  out[n] = f2bf(x1[n] + rbf(mod[5 * D + n] * a));
}

extern "C" void kernel_launch(void* const* d_in, const int* in_sizes, int n_in, void* d_out, int out_size, void* d_ws, size_t ws_size, hipStream_t stream) {
  const u16* x = (const u16*)d_in[0]; const u16* kc = (const u16*)d_in[1]; const u16* vc = (const u16*)d_in[2];
  const u16* wq = (const u16*)d_in[3]; const u16* wo = (const u16*)d_in[4];
  const u16* wg = (const u16*)d_in[6]; const u16* wu = (const u16*)d_in[7]; const u16* wd = (const u16*)d_in[8];
  const u16* c = (const u16*)d_in[9]; const u16* wmod = (const u16*)d_in[10];
  float* ws = (float*)d_ws;
  float* mod = ws; u16* q = (u16*)(ws + 4096); float* o = ws + 8192; float* x1 = ws + 16384; float* act = ws + 20480; float* part = ws + 32768;
  k_mod<<<6 * D / 64, 64, 0, stream>>>(c, wmod, mod);
  k_q<<<HQ * DH / 64, 64, 0, stream>>>(x, mod, wq, q);
  k_attn<<<dim3(NSPLIT, HKV), 256, 0, stream>>>(q, kc, vc, part);
  k_comb<<<HQ, DH, 0, stream>>>(part, o);
  k_wo<<<D / 64, 64, 0, stream>>>(o, wo, x, mod, x1);
  k_ffn1<<<F / 64, 64, 0, stream>>>(x1, mod, wg, wu, act);
  k_ffn2<<<D / 64, 64, 0, stream>>>(act, wd, x1, mod, (u16*)d_out);
}
```

```cpp
#include <hip/hip_runtime.h>
#include <hip/hip_bf16.h>
#include <stdint.h>

typedef unsigned short u16;
typedef unsigned u32;
typedef unsigned long long u64;
typedef u32 u32x2 __attribute__((ext_vector_type(2)));
typedef u32 u32x4 __attribute__((ext_vector_type(4)));
typedef float f32x2 __attribute__((ext_vector_type(2)));
typedef float f32x4 __attribute__((ext_vector_type(4)));
typedef short s16x4 __attribute__((ext_vector_type(4)));
typedef __bf16 bf16x2 __attribute__((ext_vector_type(2)));
typedef __bf16 bf16x8 __attribute__((ext_vector_type(8)));
#define GAS __attribute__((address_space(1)))
#define LAS __attribute__((address_space(3)))
#define RLX_AGENT __ATOMIC_RELAXED, __HIP_MEMORY_SCOPE_AGENT
#define RLX_WG __ATOMIC_RELAXED, __HIP_MEMORY_SCOPE_WORKGROUP

constexpr int D = 512, DH = 128, SK = 65536, FF = 1792, NQ = 4096;
constexpr int NWG = 256, NTHR = 640, NCW = 8;
constexpr int CHUNK = (SK / 32) * DH * 2;
constexpr int NTILE = CHUNK / 4096;
constexpr int NIT = NTILE / NCW;
constexpr int NF = 56;
constexpr int G = 2;
constexpr int NS = 34;
constexpr int WPUB = 24;
constexpr int WMAX = 48;
constexpr int PRE = 6;
constexpr int QAT = 2;
constexpr int ND = 4;
constexpr int T_START = 190;
constexpr int T_PACE = 3540;
constexpr int SLOW_N = 48, T_SLOW = 45;
constexpr int T_LEAD = 35;
constexpr int T_T0 = 250, T_H = 660, T_Q = 1050;
constexpr int T_ML = 600;
constexpr int RETRY = 16;
constexpr int T_H1 = 147, T_A = 172, T_B = T_A + 95, T_C = T_B + 73, T_D = T_C + 137;
constexpr int T_AGAIN = 22;
constexpr int CODE = 640;
static_assert(NS % G == 0 && WPUB % 4 == 0 && WMAX % 4 == 0 && WMAX <= 60 && ND % G == 0 && QAT >= 1 && QAT <= PRE, "geometry");
constexpr u32 TSTEP = (u32)(((u64)T_PACE << 16) / (2 * NTILE / G)), TSLOW = (u32)(((u64)T_SLOW << 16) / (SLOW_N / G));
constexpr u32 t_group(int n) { return T_START + T_SLOW + (u32)(((u64)TSTEP * n) >> 16); }
constexpr u32 T_LAST = t_group(2 * NTILE / G - 1);
constexpr u32 SPIN_LDS = 1u << 19, SPIN_MEM = 1u << 17;
constexpr u32 WS_EPOCH = 0, WS_LATE = 1024, WS_T0 = 2048, WS_READY = 4096, WS_H = 8192, WS_Q = WS_H + 8192, WS_ML = WS_Q + 8 * 4096,
              WS_MOD = WS_ML + 8 * 1024, WS_O = WS_MOD + 4096, WS_M2 = WS_O + 64 * 8192, WS_M4 = WS_M2 + 64 * 2048,
              WS_X1 = WS_M4 + 64 * 2048, WS_END = WS_X1 + 64 * 1024;
constexpr int L_CTL = 0, L_OPK = 128, L_FF = L_OPK + 128, L_ML = 512, L_MODV = L_ML + 256, L_QB = 1024, L_RED = L_QB + 1024,
              L_SC = L_RED + 2048, L_RING = L_SC + NCW * 2048, L_END = L_RING + NS * 4096;
static_assert(L_END <= 163840, "LDS budget");
enum { C_DONE = 0, C_LANDED = 8, C_CNT1 = 9, C_MRG = 10, C_QRDY = 11, C_MLCNT = 12, C_FRDY = 13, C_MODRDY = 14, C_SKEW = 16, C_LATE = 17, C_N = 18, C_BASE = 18 };

constexpr float LOG2E = 1.4426950408889634f, SCALE = 0.08838834764831845f;

#define CBAR() asm volatile("" ::: "memory")
#define SBAR() __builtin_amdgcn_sched_barrier(0)
#define LGKM0() asm volatile("s_waitcnt lgkmcnt(0)" ::: "memory")
#define VM0() asm volatile("s_waitcnt vmcnt(0)" ::: "memory")
#define SPIN(n, cap) _Pragma("unroll 1") for (u32 n = 0; n < (cap); ++n)
__device__ __forceinline__ float asf(u32 x) { return __builtin_bit_cast(float, x); }
__device__ __forceinline__ u32 asu(float x) { return __builtin_bit_cast(u32, x); }
__device__ __forceinline__ u32 pk2(float lo, float hi) { f32x2 t = {lo, hi}; return __builtin_bit_cast(u32, __builtin_convertvector(t, bf16x2)); }
__device__ __forceinline__ float lo16(u32 x) { return asf(x << 16); }
__device__ __forceinline__ float hi16(u32 x) { return asf(x & 0xffff0000u); }
__device__ __forceinline__ float rbf(float x) { return lo16(pk2(x, x)); }
__device__ __forceinline__ u32 bfbits(float x) { return pk2(x, x) & 0xffffu; }
__device__ __forceinline__ u32 canon(u32 x) { return x < 0xfffffffeu ? x : 0xfffffffeu; }
__device__ __forceinline__ u32x4 canon4(float a, float b, float c, float d) { return u32x4{canon(asu(a)), canon(asu(b)), canon(asu(c)), canon(asu(d))}; }
__device__ __forceinline__ float ex2(float x) { return __builtin_amdgcn_exp2f(x); }
__device__ __forceinline__ float rcp(float x) { return __builtin_amdgcn_rcpf(x); }
__device__ __forceinline__ float silu(float x) { return x * rcp(1.f + ex2(-LOG2E * x)); }
__device__ __forceinline__ u32 now32() { return (u32)__builtin_amdgcn_s_memrealtime(); }
__device__ __forceinline__ void until(u32 t) { while ((u32)(t - now32() - 1) < 8192u) ; }
template <int CTRL> __device__ __forceinline__ u32 dppu(u32 x) { return (u32)__builtin_amdgcn_mov_dpp((int)x, CTRL, 0xf, 0xf, true); }
template <int CTRL> __device__ __forceinline__ float dppf(float x) { return asf(dppu<CTRL>(asu(x))); }
constexpr int XOR1 = 0xB1, XOR2 = 0x4E, QB0 = 0x00, QB2 = 0xAA, HMIR = 0x141, ROR4 = 0x124, ROR8 = 0x128;
__device__ __forceinline__ float sum32(float a, float b) { auto r = __builtin_amdgcn_permlane32_swap(asu(a), asu(b), false, false); return asf(r[0]) + asf(r[1]); }
__device__ __forceinline__ float sum16(float a, float b) { auto r = __builtin_amdgcn_permlane16_swap(asu(a), asu(b), false, false); return asf(r[0]) + asf(r[1]); }
__device__ __forceinline__ float max32(float a) { auto r = __builtin_amdgcn_permlane32_swap(asu(a), asu(a), false, false); return fmaxf(asf(r[0]), asf(r[1])); }
__device__ __forceinline__ float max16(float a) { auto r = __builtin_amdgcn_permlane16_swap(asu(a), asu(a), false, false); return fmaxf(asf(r[0]), asf(r[1])); }
__device__ __forceinline__ float dot2(u32 w, u32 x, float acc) { return __builtin_amdgcn_fdot2_f32_bf16(__builtin_bit_cast(bf16x2, w), __builtin_bit_cast(bf16x2, x), acc, false); }
__device__ __forceinline__ f32x4 mfma32(u32x4 a, u32x4 b, f32x4 c) { return __builtin_amdgcn_mfma_f32_16x16x32_bf16(__builtin_bit_cast(bf16x8, a), __builtin_bit_cast(bf16x8, b), c, 0, 0, 0); }
__device__ __forceinline__ f32x4 mfma16(u32x2 a, u32x2 b, f32x4 c) { return __builtin_amdgcn_mfma_f32_16x16x16bf16_1k(__builtin_bit_cast(s16x4, a), __builtin_bit_cast(s16x4, b), c, 0, 0, 0); }
__device__ __forceinline__ u32 rfl(u32 x) { return (u32)__builtin_amdgcn_readfirstlane((int)x); }
template <class T> __device__ __forceinline__ T ldnt(const void* p) { return __builtin_nontemporal_load((const GAS T*)p); }
__device__ __forceinline__ bool bad4(u32x4 v) { return max(max(v.x, v.y), max(v.z, v.w)) == 0xffffffffu; }
__device__ __forceinline__ u32 plo(u32 r1, u32 r0) { return __builtin_amdgcn_perm(r1, r0, 0x05040100u); }
__device__ __forceinline__ u32 phi(u32 r1, u32 r0) { return __builtin_amdgcn_perm(r1, r0, 0x07060302u); }
__device__ __forceinline__ float sum8(const LAS float* p) {
  return ((p[0] + p[64]) + (p[128] + p[192])) + ((p[256] + p[320]) + (p[384] + p[448]));
}
__device__ __forceinline__ void pin(u32& v) { asm volatile("" : "+v"(v)); }
__device__ __forceinline__ void pin4(f32x4& a, f32x4& b, f32x4& c, f32x4& d) { asm volatile("" : "+v"(a), "+v"(b), "+v"(c), "+v"(d)); }
__device__ __forceinline__ void pin(u32x4& v) { asm volatile("" : "+v"(v)); }

__device__ __forceinline__ float red4_parity(float v0, float v1, float v2, float v3) {
  float t = sum16(sum32(v0, v1), sum32(v2, v3));
  t += dppf<XOR2>(t); t += dppf<ROR4>(t); t += dppf<ROR8>(t);
  return t;
}
__device__ __forceinline__ float sum_parity(float t) { t += dppf<XOR2>(t); t += dppf<ROR4>(t); t += dppf<ROR8>(t); t = sum16(t, t); return sum32(t, t); }
__device__ __forceinline__ float max_parity(float t) { t = fmaxf(t, dppf<XOR2>(t)); t = fmaxf(t, dppf<ROR4>(t)); t = fmaxf(t, dppf<ROR8>(t)); return max32(max16(t)); }
__device__ __forceinline__ void ln_stats(u32x4 v, float& mean, float& rden) {
  const float piv = lo16(rfl(v.x));
  f32x2 d[4];
#pragma unroll
  for (int i = 0; i < 4; ++i) d[i] = f32x2{lo16(v[i]), hi16(v[i])} - piv;
  const f32x2 s2 = (d[0] + d[1]) + (d[2] + d[3]), q2 = __builtin_elementwise_fma(d[0], d[0], d[1] * d[1]) + __builtin_elementwise_fma(d[2], d[2], d[3] * d[3]);
  float t = sum32(s2.x + s2.y, q2.x + q2.y);
  t = sum16(t, t);
  t += dppf<XOR1>(t); t += dppf<XOR2>(t); t += dppf<ROR4>(t); t += dppf<ROR8>(t);
  auto r = __builtin_amdgcn_permlane32_swap(asu(t), asu(t), false, false);
  const float ds = asf(r[0]) * (1.f / D), dq = asf(r[1]) * (1.f / D);
  mean = rbf(piv + ds);
  const float var = rbf(fmaxf(fmaf(-ds, ds, dq), 0.f));
  rden = rcp(rbf(__builtin_amdgcn_sqrtf(rbf(var + 1e-5f))));
}
template <int NR> __device__ __forceinline__ void pair_rows(const u32x4 (&w)[NR], u32x4 (&p)[NR]) {
#pragma unroll
  for (int i = 0; i < NR; i += 2) {
    p[i] = u32x4{plo(w[i + 1].x, w[i].x), phi(w[i + 1].x, w[i].x), plo(w[i + 1].y, w[i].y), phi(w[i + 1].y, w[i].y)};
    p[i + 1] = u32x4{plo(w[i + 1].z, w[i].z), phi(w[i + 1].z, w[i].z), plo(w[i + 1].w, w[i].w), phi(w[i + 1].w, w[i].w)};
    pin(p[i]); pin(p[i + 1]);
  }
}
template <int NR> __device__ __forceinline__ void dots(const u32x4 (&p)[NR], const u32 (&x)[NR / 2], float& w0, float& w1) {
  float v[8];
#pragma unroll
  for (int j = 0; j < 8; ++j) { v[j] = 0.f;
#pragma unroll
    for (int i = 0; i < NR / 2; ++i) v[j] = dot2(p[2 * i + (j >> 2)][j & 3], x[i], v[j]); }
  w0 = sum16(sum32(v[0], v[1]), sum32(v[2], v[3])); w1 = sum16(sum32(v[4], v[5]), sum32(v[6], v[7]));
  w0 += dppf<ROR8>(w0); w1 += dppf<ROR8>(w1);
}
__device__ __forceinline__ float gemv_wide(const u32x4 (&p)[8], const u32 (&x)[4], int lane) { float a, b; dots<8>(p, x, a, b); return (lane & 8) ? b : a; }
__device__ __forceinline__ float gemv_narrow(const u32x4 (&p)[4], const u32 (&x)[2], int lane) {
  float a, b; dots<4>(p, x, a, b); a += dppf<ROR4>(a); b += dppf<ROR4>(b); return (lane & 8) ? b : a;
}
__device__ __forceinline__ u32x4 spread8(u32 v, int lane) {
  const u32 pk = __builtin_amdgcn_perm(dppu<XOR1>(v), v, (lane & 1) ? 0x01000504u : 0x05040100u);
  const u32 a = dppu<QB0>(pk), b = dppu<QB2>(pk);
  return u32x4{a, b, dppu<HMIR>(a), dppu<HMIR>(b)};
}
__device__ __forceinline__ void frag2(u32x4 a, u32x4 b, u32x4& c0, u32x4& c1) {
  c0 = u32x4{plo(a.y, a.x), plo(a.w, a.z), plo(b.y, b.x), plo(b.w, b.z)};
  c1 = u32x4{phi(a.y, a.x), phi(a.w, a.z), phi(b.y, b.x), phi(b.w, b.z)};
  pin(c0); pin(c1);
}
__device__ __forceinline__ void frag4(u32x4 a, u32x4 b, u32x4 c, u32x4 d, u32x4 (&f)[4]) {
  frag2(u32x4{a.x, a.z, b.x, b.z}, u32x4{c.x, c.z, d.x, d.z}, f[0], f[1]);
  frag2(u32x4{a.y, a.w, b.y, b.w}, u32x4{c.y, c.w, d.y, d.w}, f[2], f[3]);
}

__global__ void __launch_bounds__(NTHR, 3)
decode_block(const u16* __restrict__ xin, const u16* __restrict__ kc, const u16* __restrict__ vc, const u16* __restrict__ wq,
             const u16* __restrict__ wo, const u16* __restrict__ wg, const u16* __restrict__ wu, const u16* __restrict__ wd,
             const u16* __restrict__ cin, const u16* __restrict__ wmod, u16* __restrict__ out, unsigned char* ws) {
  u32 junk = 0;
  if (threadIdx.x < CODE / 64) {
    u64 pc;
    asm volatile("s_getpc_b64 %1\n\tglobal_load_dword %0, %2, %1" : "+v"(junk), "=&s"(pc) : "v"((blockIdx.x >> 3) * CODE + threadIdx.x * 64));
  }
  __shared__ __attribute__((aligned(16))) unsigned char lds_raw[L_END];
  LAS unsigned char* L = (LAS unsigned char*)lds_raw;
  volatile LAS u32* ctl = (volatile LAS u32*)(L + L_CTL);
  const int lane = threadIdx.x & 63, wave = (int)rfl(threadIdx.x >> 6);
  const int b = blockIdx.x, x = b & 7, s = b >> 3, role = s >> 3, ri = s & 7;
  const int rid = 8 * x + ri;
  const int fid = 14 * (x >> 1) + 8 * (x & 1) + ri;
  const int mtask = 4 * x + (ri & 3);
  const bool isF = role == 3 && ri < 8 - 2 * (x & 1);
  const u64 kv_off = (u64)(x * 32 + s) * CHUNK;
  const auto wsr = __builtin_amdgcn_make_buffer_rsrc(ws, 0, WS_END, 0x00020000);
#define WS_LD4(off) __builtin_amdgcn_raw_buffer_load_b128(wsr, (off), 0, 16)
#define WS_LD1(off) __builtin_amdgcn_raw_buffer_load_b32(wsr, (off), 0, 16)
#define WS_ST4(v, off) __builtin_amdgcn_raw_buffer_store_b128((v), wsr, (off), 0, 16)
#define WS_ST2(v, off) __builtin_amdgcn_raw_buffer_store_b64((v), wsr, (off), 0, 16)
#define WS_ST1(v, off) __builtin_amdgcn_raw_buffer_store_b32((v), wsr, (off), 0, 16)
  auto say_late = [&](u32 ticks) { if (lane == 0) __hip_atomic_fetch_max((GAS u32*)(ws + WS_LATE), ticks, RLX_AGENT); };
  auto how_late = [&]() { CBAR(); return min(rfl(WS_LD1(WS_LATE)), 400u); };
#define WS_G(off) ((GAS u64*)(ws + (off)))
  auto wait_lds = [&](int word, u32 need) {
    SPIN(n, SPIN_LDS) { if (rfl(ctl[word]) >= need) break; __builtin_amdgcn_s_sleep(1); }
    CBAR();
  };

  if (wave == 0) {
    const u32 t0 = now32();
    ctl[C_BASE] = t0;
    const u64 ka = (u64)kc + kv_off, va = (u64)vc + kv_off;
    const u32 lbase = (u32)(uintptr_t)L + L_RING;
    const int r = lane >> 4, pos = lane & 15;
    LGKM0();
    __builtin_amdgcn_s_barrier();
    __builtin_amdgcn_s_setprio(3);
    asm volatile("s_waitcnt vmcnt(0)" : "+v"(junk));
    u32 tnext = (u32)T_START << 16, slot = 0, pub = 0, now = 0, room = NS, skew = 0;
    auto freed = [&]() {
      u32 d = ctl[C_DONE + (lane & 7)];
      d = min(d, dppu<XOR1>(d)); d = min(d, dppu<XOR2>(d)); d = min(d, dppu<HMIR>(d));
      return rfl(d);
    };
#pragma unroll 1
    for (int ph = 0; ph < 2; ++ph) {
      const u64 a = ph ? va : ka;
      const u32x4 rs = {(u32)a, (u32)(a >> 32) & 0xffffu, (u32)CHUNK, 0x00020000u};
      const int sw = ph ? 0 : 15;
      const u32 v0 = r * 256 + ((pos ^ (r & sw)) << 4), v1 = r * 256 + ((pos ^ ((4 + r) & sw)) << 4),
                v2 = r * 256 + ((pos ^ ((8 + r) & sw)) << 4), v3 = r * 256 + ((pos ^ ((12 + r) & sw)) << 4);
      const int tend = ph ? NTILE - ND : NTILE;
      int skip = ph ? NTILE - 8 * (ND - 1) - 1 : -1;
      u32 goff = 0;
#pragma unroll 1
      for (int t = 0; t < tend; t += G) {
        const u32 tile = ph * NTILE + t;
        if (!skew) { skew = rfl(ctl[C_SKEW]); tnext += (u32)max((int)skew - 1 - T_LEAD, 0) << 16; }
        const bool late = now >= (tnext >> 16);
        while (now < (tnext >> 16)) now = now32() - t0;
        if (tile == 2 * NTILE - 20) {
          const int lag = (int)(now32() - t0 - (tnext >> 16)) - (int)min(skew - 1, (u32)T_LEAD);
          if (lag > 8) say_late(lag);
        }
        tnext += tile < SLOW_N ? TSTEP + TSLOW : TSTEP;
        if (tile + G > room) {
          room = freed() + NS;
          if (tile + G > room) {
            VM0(); pub = tile; ctl[C_LANDED] = pub;
            SPIN(n, SPIN_LDS) { __builtin_amdgcn_s_sleep(1); room = freed() + NS; if (tile + G <= room) break; }
          }
        }
        u32 lag;
        if (late) { asm volatile("s_waitcnt vmcnt(%0)" ::"n"(WMAX - 4 * G) : "memory"); lag = WMAX / 4 - G; }
        else { asm volatile("s_waitcnt vmcnt(%0)" ::"n"(WPUB) : "memory"); lag = WPUB / 4; }
        if (tile > pub + lag) { pub = tile - lag; ctl[C_LANDED] = pub; }
#pragma unroll
        for (int i = 0; i < G; ++i) {
          asm volatile("s_mov_b32 m0, %0\n\ts_nop 0\n\t"
                       "buffer_load_dwordx4 %1, %5, %6 offen nt lds\n\t"
                       "buffer_load_dwordx4 %2, %5, %6 offen offset:1024 nt lds\n\t"
                       "buffer_load_dwordx4 %3, %5, %6 offen offset:2048 nt lds\n\t"
                       "buffer_load_dwordx4 %4, %5, %6 offen offset:3072 nt lds"
                       ::"s"(lbase + (slot + i) * 4096), "v"(v0), "v"(v1), "v"(v2), "v"(v3), "s"(rs),
                         "s"(goff) : "memory");
          const bool jump = t + i + 1 == skip;
          goff = rfl(goff + (jump ? 8192 : 4096)); skip += jump ? 7 : 0;
        }
        slot += G; if (slot >= NS) slot = 0;
      }
    }
#pragma unroll
    for (int k = WMAX / 4 - 1; k >= 0; --k) {
      asm volatile("s_waitcnt vmcnt(%0)" ::"n"(4 * k) : "memory");
      if (2 * NTILE - ND - k > pub) ctl[C_LANDED] = 2 * NTILE - ND - k;
    }
    return;
  }

  if (wave == 1) {
    if (lane < C_N) ctl[lane] = lane < NCW ? lane : 0;
    LGKM0();
    __builtin_amdgcn_s_barrier();
    SBAR();
    const u32 t0 = rfl(ctl[C_BASE]);
    const u64 ep = __hip_atomic_load(WS_G(WS_EPOCH), RLX_AGENT);
    const int j8 = 4 * (lane & 1) + (lane >> 5) + 2 * ((lane >> 4) & 1);
    const u32 xraw = *(const GAS u16*)(xin + 8 * rid + j8);
    const u32x4 ones = {~0u, ~0u, ~0u, ~0u};
    if (b == 0 && lane == 0) WS_ST1(0u, WS_LATE);
    if (lane < 2) WS_ST4(ones, WS_ML + x * 1024 + s * 32 + lane * 16);
    if (role == 0) {
      const u32x4 tail = lane < 48 ? ones : u32x4{0u, 0u, 0u, 0u};
      if (ri >= 4 && lane < 8) WS_ST4(ones, WS_MOD + mtask * 128 + lane * 16);
      WS_ST4(ones, WS_M2 + rid * 2048 + lane * 16); WS_ST4(ones, WS_M2 + rid * 2048 + 1024 + lane * 16);
      WS_ST4(ones, WS_M4 + rid * 2048 + lane * 16); WS_ST4(tail, WS_M4 + rid * 2048 + 1024 + lane * 16);
    } else if (role == 2) {
#pragma unroll
      for (int i = 0; i < 8; ++i) WS_ST4(ones, WS_O + rid * 8192 + i * 1024 + lane * 16);
    } else if (isF) {
      WS_ST4(ones, WS_X1 + fid * 1024 + lane * 16);
    }
    SBAR();
    const u64 tag = ep + 1;
    if (s == 0 && lane == 0) __hip_atomic_store(WS_G(WS_T0 + x * 128), (tag << 32) | t0, RLX_AGENT);
    VM0();
    if (lane == 0) __hip_atomic_store(WS_G(WS_READY + b * 8), tag, RLX_AGENT);
    u32 skew;
    {
      int d;
      for (u32 t = T_T0;; t += 60) {
        until(t0 + t);
        const u64 e = __hip_atomic_load(WS_G(WS_T0 + (lane & 7) * 128), RLX_AGENT);
        const bool ok = (u32)(e >> 32) == (u32)tag;
        d = ok ? (int)((u32)e - t0) : 0;
        if (__all(ok) || t > T_T0 + 200) break;
      }
      d = max(d, (int)dppu<XOR1>(d)); d = max(d, (int)dppu<XOR2>(d)); d = max(d, (int)dppu<HMIR>(d));
      skew = (u32)min(max((int)rfl(d), 0), 300);
      ctl[C_SKEW] = skew + 1;
    }
    const u32 tail0 = t0 + skew + T_LAST;
    if (role == 0 && ri >= 4) {
      wait_lds(C_MODRDY, 1);
      const f32x2 mv = *(const LAS f32x2*)(L + L_MODV + (lane & 31) * 8);
      if (lane < 32) WS_ST1(canon(pk2(mv.x, mv.y)), WS_MOD + mtask * 128 + lane * 4);
    }
    {
      const u32 thi = (u32)(tag >> 16), tlo = (u32)tag & 0xffffu;
      u64 g[8];
      bool rdy;
      while (now32() - t0 < T_Q + skew) __builtin_amdgcn_s_sleep(2);
      SPIN(n, SPIN_MEM) {
        bool ok = true;
#pragma unroll
        for (int i = 0; i < 8; ++i) g[i] = __hip_atomic_load(WS_G(WS_Q + x * 4096 + i * 512 + lane * 8), RLX_AGENT);
        rdy = true;
#pragma unroll
        for (int i = 0; i < 4; ++i) rdy &= __hip_atomic_load(WS_G(WS_READY + (i * 64 + lane) * 8), RLX_AGENT) == tag;
#pragma unroll
        for (int i = 0; i < 8; ++i) ok &= (u32)(g[i] >> 32) == thi && ((u32)g[i] >> 16) == tlo;
        if (__all(ok && rdy)) break;
        __builtin_amdgcn_s_sleep(RETRY);
      }
#pragma unroll
      for (int i = 0; i < 8; ++i) *(LAS u16*)(L + L_QB + i * 128 + lane * 2) = (u16)g[i];
      CBAR();
      ctl[C_QRDY] = 1;
    }
    float ga, gb;
    {
      SPIN(n, SPIN_LDS) { if (rfl(ctl[C_MLCNT]) >= NCW) break; __builtin_amdgcn_s_sleep(8); }
      CBAR();
      const f32x2 ml = *(const LAS f32x2*)(L + L_ML + (lane & 31) * 8);
      float m = fmaxf(ml.x, dppf<ROR4>(ml.x)); m = fmaxf(m, dppf<ROR8>(m)); m = max16(m);
      float l = ml.y * ex2((ml.x - m) * LOG2E);
      l += dppf<ROR4>(l); l += dppf<ROR8>(l); l = sum16(l, l);
      const u32x2 v = {canon(asu(m)), canon(asu(l))};
      if (lane < 4) WS_ST2(v, WS_ML + x * 1024 + s * 32 + lane * 8);
      const u32 t1 = now32();
      while (now32() - t1 < T_ML) __builtin_amdgcn_s_sleep(16);
      u32x4 g;
      u32 g0 = 0, g1 = 0;
      SPIN(n, SPIN_MEM) {
        CBAR();
        g = WS_LD4(WS_ML + x * 1024 + lane * 16);
        if (role == 0) { g0 = WS_LD1(WS_MOD + rid * 16 + (j8 >> 1) * 4); g1 = WS_LD1(WS_MOD + 3072 + rid * 16 + (j8 >> 1) * 4); }
        if (!__any(bad4(g) || max(g0, g1) == 0xffffffffu)) break;
        __builtin_amdgcn_s_sleep(RETRY);
      }
      ga = (j8 & 1) ? hi16(g0) : lo16(g0); gb = (j8 & 1) ? hi16(g1) : lo16(g1);
      const float ma = max_parity(asf(g.x)), mb = max_parity(asf(g.z));
      const float la = sum_parity(asf(g.y) * ex2((asf(g.x) - ma) * LOG2E)), lb = sum_parity(asf(g.w) * ex2((asf(g.z) - mb) * LOG2E));
      if (lane < 2) *(LAS f32x4*)(L + L_FF + lane * 16) = f32x4{ma, 1.f / la, mb, 1.f / lb};
      CBAR();
      ctl[C_FRDY] = 1;
    }
    if (role != 0) return;
    LGKM0();
    __builtin_amdgcn_s_barrier();
    auto gather64 = [&](u32 off, u32 t) {
      u32x4 g0, g1;
      SPIN(n, SPIN_MEM) {
        until(t); t += T_AGAIN;
        CBAR();
        g0 = WS_LD4(off); g1 = WS_LD4(off + 1024);
        if (!__any(bad4(g0) || bad4(g1))) break;
      }
      return red4_parity(asf(g0.x) + asf(g1.x), asf(g0.y) + asf(g1.y), asf(g0.z) + asf(g1.z), asf(g0.w) + asf(g1.w));
    };
    u32 o2 = WS_M2 + rid * 2048 + lane * 16, o4 = WS_M4 + rid * 2048 + lane * 16, ox = WS_X1 + lane * 1024 + rid * 16, oo = (8 * rid + j8) * 2;
    pin(o2); pin(o4); pin(ox); pin(oo);
    float xres = lo16(xraw);
    asm volatile("" : "+v"(ga), "+v"(gb), "+v"(xres));
    u32 late = rfl(ctl[C_LATE]);
    until(tail0 + late + T_B - 40);
    late = how_late();
    const float att = gather64(o2, tail0 + late + T_B);
    const float x1 = rbf(xres + rbf(ga * rbf(att)));
    {
      auto t = __builtin_amdgcn_permlane32_swap(asu(x1), asu(x1), false, false);
      const u32 pk = canon(phi(t[1], t[0]));
      auto u = __builtin_amdgcn_permlane16_swap(pk, pk, false, false);
      const u32 n0 = dppu<XOR1>(u[0]), n1 = dppu<XOR1>(u[1]);
      const u32x4 v = (lane & 1) ? u32x4{n0, n1, u[0], u[1]} : u32x4{u[0], u[1], n0, n1};
      if (lane < NF) WS_ST4(v, ox);
    }
    if (b == 0 && lane == 0) __hip_atomic_store(WS_G(WS_EPOCH), tag, RLX_AGENT);
    const float y = gather64(o4, tail0 + late + T_D);
    if ((lane & 14) == 0) *(GAS u16*)((GAS unsigned char*)out + oo) = (u16)bfbits(x1 + rbf(gb * rbf(y)));
    return;
  }

  const int w = wave - 2, g = lane >> 4, i16 = lane & 15, a8 = lane >> 3, ch = lane & 7, b2 = (lane >> 2) & 1;
  const int jcol = (lane >> 5) + 2 * ((lane >> 4) & 1) + 4 * ((lane >> 3) & 1);
  LAS float* red = (LAS float*)(L + L_RED);
  auto last_arriver = [&]() {
    CBAR();
    u32 old = 0;
    if (lane == 0) old = __hip_atomic_fetch_add((LAS u32*)(L + L_CTL) + C_CNT1, 1u, RLX_WG);
    const bool last = rfl(old) == NCW - 1;
    CBAR();
    return last;
  };
  u32x4 wp[8];
  u32x4 wu0[2];
  u64 tag = 0;
  float lnx = 0.f;
  const int r4 = 64 * w + 4 * (lane >> 2), qtask = 16 * x + (role == 3 ? 8 : 0) + ri;
  if (role == 2) {
    SBAR();
    __builtin_amdgcn_s_barrier();
    SBAR();
#pragma unroll
    for (int i = 0; i < 8; ++i) {
      const u16* p = wo + (size_t)(64 * rid + 32 * (i >> 2) + 8 * g + 2 * (i & 3)) * D + 64 * w + 4 * i16;
      const u32x2 r0 = ldnt<u32x2>(p), r1 = ldnt<u32x2>(p + D);
      wp[i] = u32x4{r0.x, r0.y, r1.x, r1.y};
    }
  } else if (role == 0 && ri >= 4) {
    u32x4 wn[8];
    SBAR();
    __builtin_amdgcn_s_barrier();
    SBAR();
#pragma unroll
    for (int i = 0; i < 8; ++i) wn[i] = ldnt<u32x4>(wmod + (size_t)(64 * w + 8 * a8 + i) * (6 * D) + 2 * D + 64 * mtask + 8 * ch);
    SBAR();
    const u32x4 craw = *(const GAS u32x4*)(cin + 64 * w + 8 * a8);
    SBAR();
    u32 xs[4];
#pragma unroll
    for (int i = 0; i < 4; ++i) xs[i] = pk2(silu(lo16(craw[i])), silu(hi16(craw[i])));
    pair_rows<8>(wn, wp);
    red[w * 64 + 8 * ch + jcol] = gemv_wide(wp, xs, lane);
    if (last_arriver()) {
      float t = sum8(red + lane);
      t = rbf(t);
      if ((mtask >> 3) == 2) t = rbf(1.f + t);
      ((LAS float*)(L + L_MODV))[lane] = t;
      CBAR();
      ctl[C_MODRDY] = 1;
    }
  } else if (role != 0) {
    u32x4 wn[4];
    const u16* p0 = wq + (size_t)r4 * NQ + 32 * qtask + 8 * (lane & 3);
    wn[0] = ldnt<u32x4>(p0); wn[1] = ldnt<u32x4>(p0 + NQ);
    SBAR();
    __builtin_amdgcn_s_barrier();
    SBAR();
    wn[2] = ldnt<u32x4>(p0 + 2 * NQ); wn[3] = ldnt<u32x4>(p0 + 3 * NQ);
    SBAR();
    const u32x4 xv = *(const GAS u32x4*)(xin + lane * 8);
    const u32 xraw = *(const GAS u16*)(xin + 64 * w + lane);
    const u64 ep = __hip_atomic_load(WS_G(WS_EPOCH), RLX_AGENT);
    SBAR();
    if (isF) {
      const u16* p = wg + (size_t)(64 * w + 8 * g) * FF + 32 * fid + 2 * i16;
#pragma unroll
      for (int i = 0; i < 4; ++i) {
        const u16* q = p + (32 * (i >> 1) + 4 * (i & 1)) * FF;
        wp[4 + i] = u32x4{ldnt<u32>(q), ldnt<u32>(q + FF), ldnt<u32>(q + 2 * FF), ldnt<u32>(q + 3 * FF)};
      }
#pragma unroll
      for (int i = 0; i < 2; ++i) {
        const u16* q = wu + (p - wg) + 4 * i * FF;
        wu0[i] = u32x4{ldnt<u32>(q), ldnt<u32>(q + FF), ldnt<u32>(q + 2 * FF), ldnt<u32>(q + 3 * FF)};
      }
    }
    SBAR();
    float mean, rden;
    ln_stats(xv, mean, rden);
    lnx = rbf(rbf(lo16(xraw) - mean) * rden);
    asm volatile("" : "+v"(lnx));
    tag = (((u64)rfl((u32)(ep >> 32)) << 32) | rfl((u32)ep)) + 1;
    pair_rows<4>(wn, (u32x4 (&)[4])wp[0]);
  } else {
    u32x4 wn[4], wp2[4];
    const u16* p = wmod + (size_t)r4 * (6 * D) + 32 * mtask + 8 * (lane & 3);
    wn[0] = ldnt<u32x4>(p); wn[1] = ldnt<u32x4>(p + 6 * D);
    SBAR();
    __builtin_amdgcn_s_barrier();
    SBAR();
    wn[2] = ldnt<u32x4>(p + 12 * D); wn[3] = ldnt<u32x4>(p + 18 * D);
    SBAR();
    const u32x2 craw = *(const GAS u32x2*)(cin + r4);
    const u64 ep = __hip_atomic_load(WS_G(WS_EPOCH), RLX_AGENT);
    SBAR();
    const u32 xs[2] = {pk2(silu(lo16(craw.x)), silu(hi16(craw.x))), pk2(silu(lo16(craw.y)), silu(hi16(craw.y)))};
    pair_rows<4>(wn, wp2);
    red[w * 64 + 8 * (lane & 3) + jcol] = gemv_narrow(wp2, xs, lane);
    if (last_arriver()) {
      float t = sum8(red + (lane & 31));
      t = rbf(t);
      if (mtask >= 16) t = rbf(1.f + t);
      if (lane < 32) __hip_atomic_store(WS_G(WS_H + (32 * mtask + lane) * 8), ((ep + 1) << 16) | (asu(t) >> 16), RLX_AGENT);
    }
  }
  const u32 tw0 = rfl(ctl[C_BASE]);
  auto q_task = [&]() {
    u64 sg, cg;
    wait_lds(C_SKEW, 1);
    const u32 th = T_H + rfl(ctl[C_SKEW]);
    while (now32() - tw0 < th) __builtin_amdgcn_s_sleep(1);
    SPIN(n, SPIN_MEM) {
      sg = __hip_atomic_load(WS_G(WS_H + (64 * w + lane) * 8), RLX_AGENT);
      cg = __hip_atomic_load(WS_G(WS_H + 4096 + (64 * w + lane) * 8), RLX_AGENT);
      if (__all((sg >> 16) == (tag & 0xffffffffffffull) && (cg >> 16) == (tag & 0xffffffffffffull))) break;
      __builtin_amdgcn_s_sleep(RETRY / 2);
    }
    const u32 h = pk2(rbf(lnx * lo16((u32)cg)) + lo16((u32)sg), 0.f);
    const u32 pk = __builtin_amdgcn_perm(dppu<XOR1>(h), h, (lane & 1) ? 0x01000504u : 0x05040100u);
    const u32 xs[2] = {dppu<QB0>(pk), dppu<QB2>(pk)};
    red[w * 64 + 8 * (lane & 3) + jcol] = gemv_narrow((const u32x4 (&)[4])wp[0], xs, lane);
    if (last_arriver()) {
      float t = sum8(red + (lane & 31));
      if (lane < 32) __hip_atomic_store(WS_G(WS_Q + (32 * qtask + lane) * 8), (tag << 16) | bfbits(t), RLX_AGENT);
    }
  };

  const int head = i16 >> 2;
  LAS unsigned char* sc = L + L_SC + w * 2048;
  u32 slot = w;
  auto fetch = [&](u32 pos, u32 o0, u32 o1, u32 o2, u32 o3, u32x4 (&f)[4]) {
    wait_lds(C_LANDED, pos + 1);
    const LAS unsigned char* p = L + L_RING + slot * 4096;
    f[0] = *(const LAS u32x4*)(p + o0); f[1] = *(const LAS u32x4*)(p + o1); f[2] = *(const LAS u32x4*)(p + o2); f[3] = *(const LAS u32x4*)(p + o3);
    SBAR();
    asm volatile("s_waitcnt lgkmcnt(0)" : "+v"(f[0]), "+v"(f[1]), "+v"(f[2]), "+v"(f[3]) :: "memory");
    ctl[C_DONE + w] = pos + NCW;
    slot += NCW; if (slot >= NS) slot -= NS;
  };
  float m_w;
  {
    const u32 ko = i16 * 256 + ((g ^ i16) << 4);
    static_assert(QAT >= 1 && QAT <= PRE, "the Q task runs between two parked tiles");
    u32x4 pre[PRE][4];
#pragma unroll
    for (int i = 0; i < PRE; ++i) {
      fetch(i * NCW + w, ko, ko ^ 64, ko ^ 128, ko ^ 192, pre[i]);
      if (i == QAT - 1 && (role & 1)) { q_task(); wp[0] = wu0[0]; wp[1] = wu0[1]; pin(wp[0]); pin(wp[1]); }
    }
    wait_lds(C_QRDY, 1);
    u32x4 qf[4];
#pragma unroll
    for (int ks = 0; ks < 4; ++ks) qf[ks] = *(const LAS u32x4*)(L + L_QB + head * 256 + ks * 64 + g * 16);
    float mx = -INFINITY;
    auto score = [&](int i, const u32x4 (&kf)[4]) {
      f32x4 d = {0.f, 0.f, 0.f, 0.f};
#pragma unroll
      for (int ks = 0; ks < 4; ++ks) d = mfma32(kf[ks], qf[ks], d);
      mx = fmaxf(fmaxf(mx, fmaxf(d.x, d.y)), fmaxf(d.z, d.w));
      const u32 r0 = pk2(d.x, d.y), r1 = pk2(d.z, d.w);
      const u32x2 z = {pk2(lo16(r0) * SCALE, hi16(r0) * SCALE), pk2(lo16(r1) * SCALE, hi16(r1) * SCALE)};
      if ((lane & 3) == 0) *(LAS u32x2*)(sc + i * 128 + g * 32 + head * 8) = z;
    };
#pragma unroll
    for (int i = 0; i < PRE; ++i) score(i, pre[i]);
#pragma unroll 1
    for (int i = PRE; i < NIT; ++i) {
      u32x4 kf[4];
      fetch(i * NCW + w, ko, ko ^ 64, ko ^ 128, ko ^ 192, kf);
      score(i, kf);
    }
    m_w = rbf(rbf(max32(max16(mx))) * SCALE);
  }
  __builtin_amdgcn_s_waitcnt(0x0F70);
  {
    const u32 mu = asu(m_w);
    const float m0 = asf(__builtin_amdgcn_readlane(mu, 0)), m1 = asf(__builtin_amdgcn_readlane(mu, 4)),
                m2 = asf(__builtin_amdgcn_readlane(mu, 8)), m3 = asf(__builtin_amdgcn_readlane(mu, 12));
    const float ma = (lane & 1) ? m2 : m0, mb = (lane & 1) ? m3 : m1;
    float la = 0.f, lb = 0.f;
    CBAR();
#pragma unroll
    for (int k = 0; k < 2; ++k) {
      u32x4 v = *(const LAS u32x4*)(sc + k * 1024 + lane * 16);
#pragma unroll
      for (int d = 0; d < 4; ++d) {
        const float mm = d < 2 ? ma : mb;
        const float p0 = ex2((lo16(v[d]) - mm) * LOG2E), p1 = ex2((hi16(v[d]) - mm) * LOG2E);
        if (d < 2) la += p0 + p1; else lb += p0 + p1;
        v[d] = pk2(p0, p1);
      }
      *(LAS u32x4*)(sc + k * 1024 + lane * 16) = v;
    }
    la = sum_parity(la); lb = sum_parity(lb);
    if (lane < 2) *(LAS f32x4*)(L + L_ML + w * 32 + lane * 16) = f32x4{ma, la, mb, lb};
    CBAR();
    if (lane == 0) __hip_atomic_fetch_add((LAS u32*)(L + L_CTL) + C_MLCNT, 1u, RLX_WG);
    m_w = g == 0 ? m0 : g == 1 ? m1 : g == 2 ? m2 : m3;
  }
  f32x4 acc0 = {0.f, 0.f, 0.f, 0.f}, acc1 = {0.f, 0.f, 0.f, 0.f};
  const int js = lane & 3;
  const u32 sx0 = js == 0 ? 0x0c0c0100u : js == 1 ? 0x01000c0cu : 0x0c0c0c0cu, sx1 = js == 0 ? 0x0c0c0302u : js == 1 ? 0x03020c0cu : 0x0c0c0c0cu;
  const u32 sy0 = js == 2 ? 0x0c0c0100u : js == 3 ? 0x01000c0cu : 0x0c0c0c0cu, sy1 = js == 2 ? 0x0c0c0302u : js == 3 ? 0x03020c0cu : 0x0c0c0c0cu;
  const u32 vo = g * 1024 + i16 * 16;
  auto p_frags = [&](int i, u32x2 (&pa)[4]) {
    const u32x2 p = *(const LAS u32x2*)(sc + i * 128 + g * 32 + head * 8);
#pragma unroll
    for (int t = 0; t < 4; ++t) pa[t] = u32x2{__builtin_amdgcn_perm(0u, p[t >> 1], (t & 1) ? sx1 : sx0), __builtin_amdgcn_perm(0u, p[t >> 1], (t & 1) ? sy1 : sy0)};
  };
  auto pv = [&](const u32x2 (&pa)[4], const u32x4 (&vf)[4]) {
#pragma unroll
    for (int t = 0; t < 4; ++t) { acc0 = mfma16(pa[t], u32x2{vf[t].x, vf[t].y}, acc0); acc1 = mfma16(pa[t], u32x2{vf[t].z, vf[t].w}, acc1); }
  };
  auto vtile = [&](int i, u32 pos) {
    u32x2 pa[4]; u32x4 vf[4];
    p_frags(i, pa);
    fetch(pos, vo, vo + 256, vo + 512, vo + 768, vf);
    pv(pa, vf);
  };
#pragma unroll 1
  for (int i = 0; i < NIT - ND; ++i) vtile(i, NTILE + i * NCW + w);
  wait_lds(C_FRDY, 1);
  const f32x2 ff = *(const LAS f32x2*)(L + L_FF + g * 8);
  float fac = ex2((m_w - ff.x) * LOG2E) * ff.y;
  asm volatile("" : "+v"(fac));
  if (role == 2) {
    u32x4 f0[4], f1[4];
    frag4(wp[0], wp[1], wp[2], wp[3], f0); frag4(wp[4], wp[5], wp[6], wp[7], f1);
#pragma unroll
    for (int j = 0; j < 4; ++j) { wp[j] = f0[j]; wp[4 + j] = f1[j]; }
  } else if (isF) {
#pragma unroll
    for (int j = 4; j < 8; j += 2) { u32x4 c0, c1; frag2(wp[j], wp[j + 1], c0, c1); wp[j] = c0; wp[j + 1] = c1; }
    { u32x4 c0, c1; frag2(wp[0], wp[1], c0, c1); wp[0] = c0; wp[1] = c1; }
  }
  const u32 skew = rfl(ctl[C_SKEW]) - 1, tailw = tw0 + skew + T_LAST;
  const int frow = 64 * w + 32 * (a8 & 1) + 8 * g + ch;
  u32 c1v = 0, shv = 0;
  if (isF) {
    SPIN(n, SPIN_MEM) {
      CBAR();
      c1v = WS_LD1(WS_MOD + 2048 + (frow >> 1) * 4); shv = WS_LD1(WS_MOD + 1024 + (frow >> 1) * 4);
      if (!__any(max(c1v, shv) == 0xffffffffu)) break;
      __builtin_amdgcn_s_sleep(RETRY);
    }
  }
  if (w < NCW - 1) {
#pragma unroll 1
    for (int i = NIT - ND; i < NIT; ++i) {
      vtile(i, NTILE + i * NCW + w - (i - (NIT - ND)));
      slot = slot ? slot - 1 : NS - 1;
    }
    *(LAS f32x4*)(sc + g * 512 + i16 * 32) = acc0 * fac;
    *(LAS f32x4*)(sc + g * 512 + i16 * 32 + 16) = acc1 * fac;
    CBAR();
    if (lane == 0) __hip_atomic_fetch_add((LAS u32*)(L + L_CTL) + C_MRG, 1u, RLX_WG);
    if (w == 0 && role != 1) { ctl[C_LATE] = how_late(); LGKM0(); }
  } else {
    u32 off = WS_O + (8 * x + 2 * g + (i16 >> 3)) * 8192 + ch * 1024 + s * 32;
    pin(off);
    u32x2 pa[2][4];
    u32x4 va[ND][4];
    const unsigned char* vp = (const unsigned char*)vc + kv_off + vo;

#pragma unroll
    for (int k = 0; k < ND; ++k) {
      if (k % G == 0) until(tw0 + max((int)skew - T_LEAD, 0) + t_group((2 * NTILE - ND + k) / G));
#pragma unroll
      for (int t = 0; t < 4; ++t) va[k][t] = ldnt<u32x4>(vp + (NTILE - 1 - 8 * (ND - 1 - k)) * 4096 + t * 256);
      SBAR();
    }
#pragma unroll
    for (int k = 0; k < ND - 2; ++k) { p_frags(NIT - ND + k, pa[0]); pv(pa[0], va[k]); }
    p_frags(NIT - 2, pa[0]); p_frags(NIT - 1, pa[1]);
    asm volatile("" : "+v"(pa[0][0]), "+v"(pa[0][1]), "+v"(pa[0][2]), "+v"(pa[0][3]), "+v"(pa[1][0]), "+v"(pa[1][1]), "+v"(pa[1][2]), "+v"(pa[1][3]), "+v"(acc0), "+v"(acc1));
    SBAR();
    SPIN(n, SPIN_LDS) { if (rfl(ctl[C_MRG]) >= NCW - 1) break; }
    CBAR();
    f32x4 o0 = {0.f, 0.f, 0.f, 0.f}, o1 = {0.f, 0.f, 0.f, 0.f};
#pragma unroll
    for (int i = 0; i < NCW - 1; ++i) {
      o0 += *(const LAS f32x4*)(L + L_SC + i * 2048 + g * 512 + i16 * 32); o1 += *(const LAS f32x4*)(L + L_SC + i * 2048 + g * 512 + i16 * 32 + 16);
    }
    asm volatile("" : "+v"(o0), "+v"(o1));
    SBAR();
    pv(pa[0], va[ND - 2]); pv(pa[1], va[ND - 1]);
    o0 += acc0 * fac; o1 += acc1 * fac;
    WS_ST4(canon4(o0.x, o0.y, o0.z, o0.w), off); WS_ST4(canon4(o1.x, o1.y, o1.z, o1.w), off + 16);
    const int d = (int)(now32() - tailw) - T_H1;
    if (d > 0) say_late(d + 25);
  }

  if (role == 2) {
    u32 oi = WS_O + rid * 8192 + w * 1024 + lane * 16, os = WS_M2 + (8 * w + (i16 >> 1)) * 2048 + rid * 32 + (lane & 1) * 16,
        ol = L_OPK + w * 16 + (4 * (lane & 1) + (lane >> 5) + 2 * ((lane >> 4) & 1)) * 2, or_ = L_OPK + g * 16;
    pin(oi); pin(os); pin(ol); pin(or_);
    __builtin_amdgcn_s_barrier();
    const u32 ta = tailw + rfl(ctl[C_LATE]) + T_A;
    u32x4 v;
    SPIN(n, SPIN_MEM) {
      until(ta + n * T_AGAIN);
      CBAR();
      v = WS_LD4(oi);
      if (!__any(bad4(v))) break;
    }
    const float o = red4_parity(asf(v.x), asf(v.y), asf(v.z), asf(v.w));
    if ((lane & 14) == 0) *(LAS u16*)(L + ol) = (u16)bfbits(o);
    LGKM0();
    __builtin_amdgcn_s_barrier();
    CBAR();
    const u32x4 x0 = *(const LAS u32x4*)(L + or_), x1 = *(const LAS u32x4*)(L + or_ + 64);
    f32x4 d[4];
#pragma unroll
    for (int j = 0; j < 4; ++j) d[j] = mfma32(x0, wp[j], f32x4{0.f, 0.f, 0.f, 0.f});
#pragma unroll
    for (int j = 0; j < 4; ++j) d[j] = mfma32(x1, wp[4 + j], d[j]);
    pin4(d[0], d[1], d[2], d[3]);
    if (lane < 16) WS_ST4(canon4(d[0].x, d[1].x, d[2].x, d[3].x), os);
  } else if (isF) {
#define DMA1 "s_mov_b32 m0, %3\n\ts_nop 0\n\tbuffer_load_dwordx4 %1, %0, %2 offen nt lds"
    const u32 lb = L_RING + w * 8192, m = rfl((u32)(uintptr_t)L + lb);
    const u64 ua = (u64)wu + 64 * fid, da = (u64)wd + (u64)fid * 32 * D * 2;
    const u32x4 ru = {(u32)ua, (u32)(ua >> 32) & 0xffffu, D * FF * 2 - 64u * fid, 0x00020000u}, rd = {(u32)da, (u32)(da >> 32) & 0xffffu, 32 * D * 2, 0x00020000u};
    const u32x4 rw = {(u32)(uintptr_t)ws, (u32)((uintptr_t)ws >> 32) & 0xffffu, WS_END, 0x00020000u};
    const u32 vu = (64 * w + 2 * (lane >> 2)) * (FF * 2) + (lane & 3) * 16, vd = a8 * (D * 2) + w * 128 + ch * 16;
    __builtin_amdgcn_s_barrier();
#pragma unroll
    for (int i = 1; i < 4; i += 2) asm volatile(DMA1 ::"s"(ru), "v"(vu), "s"((32 * (i & 1) + (i >> 1)) * FF * 2), "s"(m + i * 1024) : "memory");
#pragma unroll
    for (int i = 0; i < 4; ++i) asm volatile(DMA1 ::"s"(rd), "v"(vd), "s"(i * 8 * D * 2), "s"(m + 4096 + i * 1024) : "memory");
    u32 lraw;
    asm volatile("buffer_load_dword %0, off, %1, 0 offset:%2 sc1" : "=v"(lraw) : "s"(rw), "n"(WS_LATE) : "memory");
    const u32 sh16 = (ch & 1) ? 0u : 16u;
    float c1 = hi16(c1v << sh16), sh = hi16(shv << sh16);
    asm volatile("s_waitcnt vmcnt(5)" : "+v"(c1), "+v"(sh) :: "memory");
#pragma unroll
    for (int ks = 1; ks < 2; ++ks) {
      const LAS u32* e = (const LAS u32*)(L + lb + ks * 1024 + g * 256 + i16 * 4);
      frag2(u32x4{e[0], e[512], e[16], e[528]}, u32x4{e[32], e[544], e[48], e[560]}, wp[2 * ks], wp[2 * ks + 1]);
    }
    u32 oa = WS_X1 + fid * 1024 + lane * 16, ob = WS_X1 + fid * 1024 + (frow >> 3) * 16 + (ch >> 1) * 4;
    u32 os = WS_M4 + (8 * w + (i16 >> 1)) * 2048 + fid * 32 + (lane & 1) * 16, ol = L_RED + w * 256 + lane * 16,
        or_ = L_RED + (16 * g + 4 * (ch >> 1) + (ch & 1) + 2 * (a8 & 1)) * 4;
    pin(oa); pin(ob); pin(os); pin(ol); pin(or_);
    asm volatile("s_waitcnt vmcnt(0)" : "+v"(lraw) :: "memory");
    auto col4 = [&](u32 o) { const LAS u32x2* p = (const LAS u32x2*)(L + o + g * 1024 + i16 * 8); const u32x2 r0 = p[0], r1 = p[16]; return u32x4{r0.x, r0.y, r1.x, r1.y}; };
    u32x4 fd[4];
    frag4(col4(lb + 4096), col4(lb + 4352), col4(lb + 4608), col4(lb + 4864), fd);
    const u32 late = min(rfl(lraw), 400u);
    u32x4 xa; u32 xb;
    SPIN(n, SPIN_MEM) {
      until(tailw + late + T_C + n * T_AGAIN);
      CBAR();
      xa = WS_LD4(oa); xb = WS_LD1(ob);
      if (!__any(bad4(xa) || xb == 0xffffffffu)) break;
    }
    float mean, rden;
    ln_stats(xa, mean, rden);
    const u32x4 x0 = spread8(pk2(rbf(rbf(rbf(hi16(xb << sh16) - mean) * rden) * c1) + sh, 0.f), lane);
    const u32x4 x1 = {dppu<ROR8>(x0.x), dppu<ROR8>(x0.y), dppu<ROR8>(x0.z), dppu<ROR8>(x0.w)};
    const f32x4 z = {0.f, 0.f, 0.f, 0.f};
    f32x4 d0 = mfma32(x0, wp[4], z), d1 = mfma32(x0, wp[5], z), d2 = mfma32(x0, wp[0], z), d3 = mfma32(x0, wp[1], z);
    d0 = mfma32(x1, wp[6], d0); d1 = mfma32(x1, wp[7], d1); d2 = mfma32(x1, wp[2], d2); d3 = mfma32(x1, wp[3], d3);
    pin4(d0, d1, d2, d3);
    if (lane < 16) *(LAS f32x4*)(L + ol) = f32x4{d0.x, d1.x, d2.x, d3.x};
    LGKM0();
    __builtin_amdgcn_s_barrier();
    CBAR();
    const float t = rbf(sum8((const LAS float*)(L + or_)));
    const float o = dppf<ROR8>(t);
    u32x4 act = spread8(bfbits(rbf(silu((a8 & 1) ? o : t)) * ((a8 & 1) ? t : o)), lane);
    f32x4 d[4];
#pragma unroll
    for (int j = 0; j < 4; ++j) d[j] = mfma32(act, fd[j], z);
    pin4(d[0], d[1], d[2], d[3]);
    if (lane < 16) WS_ST4(canon4(d[0].x, d[1].x, d[2].x, d[3].x), os);
  } else if (role == 0) {
    __builtin_amdgcn_s_barrier();
  }
}

extern "C" void kernel_launch(void* const* d_in, const int* in_sizes, int n_in, void* d_out, int out_size, void* d_ws,
                              size_t ws_size, hipStream_t stream) {
  decode_block<<<NWG, NTHR, 0, stream>>>((const u16*)d_in[0], (const u16*)d_in[1], (const u16*)d_in[2], (const u16*)d_in[3],
                                         (const u16*)d_in[4], (const u16*)d_in[6], (const u16*)d_in[7], (const u16*)d_in[8],
                                         (const u16*)d_in[9], (const u16*)d_in[10], (u16*)d_out, (unsigned char*)d_ws);
}
```

```cpp
#include <hip/hip_runtime.h>
#include <hip/hip_bf16.h>
#include <stdint.h>

typedef unsigned short u16;
typedef unsigned u32;
typedef unsigned long long u64;
typedef u32 u32x2 __attribute__((ext_vector_type(2)));
typedef u32 u32x4 __attribute__((ext_vector_type(4)));
typedef float f32x2 __attribute__((ext_vector_type(2)));
typedef float f32x4 __attribute__((ext_vector_type(4)));
typedef short s16x4 __attribute__((ext_vector_type(4)));
typedef __bf16 bf16x2 __attribute__((ext_vector_type(2)));
typedef __bf16 bf16x8 __attribute__((ext_vector_type(8)));
#define GAS __attribute__((address_space(1)))
#define LAS __attribute__((address_space(3)))
#define RLX_AGENT __ATOMIC_RELAXED, __HIP_MEMORY_SCOPE_AGENT
#define RLX_WG __ATOMIC_RELAXED, __HIP_MEMORY_SCOPE_WORKGROUP

constexpr int D = 512, DH = 128, SK = 65536, FF = 1792, NQ = 4096;
constexpr int NWG = 256, NTHR = 640, NCW = 8;
constexpr int CHUNK = (SK / 32) * DH * 2;
constexpr int NTILE = CHUNK / 4096;
constexpr int NIT = NTILE / NCW;
constexpr int NF = 56;
constexpr int G = 2;
constexpr int NS = 34;
constexpr int WPUB = 24;
constexpr int WMAX = 56;
constexpr int PRE = 6;
constexpr int QAT = 2;
constexpr int ND = 4;
constexpr int T_START = 185;
constexpr int T_PACE = 3510;
constexpr int SLOW_N = 40, T_SLOW = 60;
constexpr int T_LEAD = 35;
constexpr int T_CATCH = 15;
constexpr int T_T0 = 250, T_H = 670, T_Q = 1060;
constexpr int T_ML = 600;
constexpr int RETRY = 16;
constexpr int T_H1 = 150, T_A = 175, T_B = T_A + 95, T_C = T_B + 73, T_D = T_C + 137;
constexpr int T_AGAIN = 22;
constexpr int CODE = 640;
static_assert(NS % G == 0 && WPUB % 4 == 0 && WMAX % 4 == 0 && WMAX <= 60 && ND % G == 0 && QAT >= 1 && QAT <= PRE, "geometry");
constexpr u32 TSTEP = (u32)(((u64)T_PACE << 16) / (2 * NTILE / G)), TSLOW = (u32)(((u64)T_SLOW << 16) / (SLOW_N / G));
constexpr u32 t_group(int n) { return T_START + T_SLOW + (u32)(((u64)TSTEP * n) >> 16); }
constexpr u32 T_LAST = t_group(2 * NTILE / G - 1);
constexpr int SKEW0 = 512;
__device__ constexpr int stream_shift(int skew) { return skew > T_LEAD ? skew - T_LEAD : skew < 0 ? skew : 0; }
constexpr u32 SPIN_LDS = 1u << 19, SPIN_MEM = 1u << 17;
constexpr u32 WS_EPOCH = 0, WS_LATE = 1024, WS_T0 = 2048, WS_READY = 4096, WS_H = 8192, WS_Q = WS_H + 8192, WS_ML = WS_Q + 8 * 4096,
              WS_MOD = WS_ML + 8 * 1024, WS_O = WS_MOD + 4096, WS_M2 = WS_O + 64 * 8192, WS_M4 = WS_M2 + 64 * 2048,
              WS_X1 = WS_M4 + 64 * 2048, WS_END = WS_X1 + 64 * 1024;
constexpr int L_CTL = 0, L_OPK = 128, L_FF = L_OPK + 128, L_ML = 512, L_MODV = L_ML + 256, L_QB = 1024, L_RED = L_QB + 1024,
              L_SC = L_RED + 2048, L_RING = L_SC + NCW * 2048, L_END = L_RING + NS * 4096;
static_assert(L_END <= 163840, "LDS budget");
enum { C_DONE = 0, C_LANDED = 8, C_CNT1 = 9, C_MRG = 10, C_QRDY = 11, C_MLCNT = 12, C_FRDY = 13, C_MODRDY = 14, C_SKEW = 16, C_N = 17, C_BASE = 17 };

constexpr float LOG2E = 1.4426950408889634f, SCALE = 0.08838834764831845f;

#define CBAR() asm volatile("" ::: "memory")
#define SBAR() __builtin_amdgcn_sched_barrier(0)
#define LGKM0() asm volatile("s_waitcnt lgkmcnt(0)" ::: "memory")
#define VM0() asm volatile("s_waitcnt vmcnt(0)" ::: "memory")
#define SPIN(n, cap) _Pragma("unroll 1") for (u32 n = 0; n < (cap); ++n)
__device__ __forceinline__ float asf(u32 x) { return __builtin_bit_cast(float, x); }
__device__ __forceinline__ u32 asu(float x) { return __builtin_bit_cast(u32, x); }
__device__ __forceinline__ u32 pk2(float lo, float hi) { f32x2 t = {lo, hi}; return __builtin_bit_cast(u32, __builtin_convertvector(t, bf16x2)); }
__device__ __forceinline__ float lo16(u32 x) { return asf(x << 16); }
__device__ __forceinline__ float hi16(u32 x) { return asf(x & 0xffff0000u); }
__device__ __forceinline__ float rbf(float x) { return lo16(pk2(x, x)); }
__device__ __forceinline__ u32 bfbits(float x) { return pk2(x, x) & 0xffffu; }
__device__ __forceinline__ u32 canon(u32 x) { return x < 0xfffffffeu ? x : 0xfffffffeu; }
__device__ __forceinline__ u32x4 canon4(float a, float b, float c, float d) { return u32x4{canon(asu(a)), canon(asu(b)), canon(asu(c)), canon(asu(d))}; }
__device__ __forceinline__ float ex2(float x) { return __builtin_amdgcn_exp2f(x); }
__device__ __forceinline__ float rcp(float x) { return __builtin_amdgcn_rcpf(x); }
__device__ __forceinline__ float silu(float x) { return x * rcp(1.f + ex2(-LOG2E * x)); }
__device__ __forceinline__ u32 now32() { return (u32)__builtin_amdgcn_s_memrealtime(); }
__device__ __forceinline__ void until(u32 t) { while ((u32)(t - now32() - 1) < 8192u) ; }
template <int CTRL> __device__ __forceinline__ u32 dppu(u32 x) { return (u32)__builtin_amdgcn_mov_dpp((int)x, CTRL, 0xf, 0xf, true); }
template <int CTRL> __device__ __forceinline__ float dppf(float x) { return asf(dppu<CTRL>(asu(x))); }
constexpr int XOR1 = 0xB1, XOR2 = 0x4E, QB0 = 0x00, QB2 = 0xAA, HMIR = 0x141, ROR4 = 0x124, ROR8 = 0x128;
__device__ __forceinline__ float sum32(float a, float b) { auto r = __builtin_amdgcn_permlane32_swap(asu(a), asu(b), false, false); return asf(r[0]) + asf(r[1]); }
__device__ __forceinline__ float sum16(float a, float b) { auto r = __builtin_amdgcn_permlane16_swap(asu(a), asu(b), false, false); return asf(r[0]) + asf(r[1]); }
__device__ __forceinline__ float max32(float a) { auto r = __builtin_amdgcn_permlane32_swap(asu(a), asu(a), false, false); return fmaxf(asf(r[0]), asf(r[1])); }
__device__ __forceinline__ float max16(float a) { auto r = __builtin_amdgcn_permlane16_swap(asu(a), asu(a), false, false); return fmaxf(asf(r[0]), asf(r[1])); }
__device__ __forceinline__ float dot2(u32 w, u32 x, float acc) { return __builtin_amdgcn_fdot2_f32_bf16(__builtin_bit_cast(bf16x2, w), __builtin_bit_cast(bf16x2, x), acc, false); }
__device__ __forceinline__ f32x4 mfma32(u32x4 a, u32x4 b, f32x4 c) { return __builtin_amdgcn_mfma_f32_16x16x32_bf16(__builtin_bit_cast(bf16x8, a), __builtin_bit_cast(bf16x8, b), c, 0, 0, 0); }
__device__ __forceinline__ f32x4 mfma16(u32x2 a, u32x2 b, f32x4 c) { return __builtin_amdgcn_mfma_f32_16x16x16bf16_1k(__builtin_bit_cast(s16x4, a), __builtin_bit_cast(s16x4, b), c, 0, 0, 0); }
__device__ __forceinline__ u32 rfl(u32 x) { return (u32)__builtin_amdgcn_readfirstlane((int)x); }
template <class T> __device__ __forceinline__ T ldnt(const void* p) { return __builtin_nontemporal_load((const GAS T*)p); }
__device__ __forceinline__ bool bad4(u32x4 v) { return max(max(v.x, v.y), max(v.z, v.w)) == 0xffffffffu; }
__device__ __forceinline__ u32 plo(u32 r1, u32 r0) { return __builtin_amdgcn_perm(r1, r0, 0x05040100u); }
__device__ __forceinline__ u32 phi(u32 r1, u32 r0) { return __builtin_amdgcn_perm(r1, r0, 0x07060302u); }
__device__ __forceinline__ float sum8(const LAS float* p) {
  return ((p[0] + p[64]) + (p[128] + p[192])) + ((p[256] + p[320]) + (p[384] + p[448]));
}
__device__ __forceinline__ void pin(u32& v) { asm volatile("" : "+v"(v)); }
__device__ __forceinline__ void pin4(f32x4& a, f32x4& b, f32x4& c, f32x4& d) { asm volatile("" : "+v"(a), "+v"(b), "+v"(c), "+v"(d)); }
__device__ __forceinline__ void pin(u32x4& v) { asm volatile("" : "+v"(v)); }

__device__ __forceinline__ float red4_parity(float v0, float v1, float v2, float v3) {
  float t = sum16(sum32(v0, v1), sum32(v2, v3));
  t += dppf<XOR2>(t); t += dppf<ROR4>(t); t += dppf<ROR8>(t);
  return t;
}
__device__ __forceinline__ float sum_parity(float t) { t += dppf<XOR2>(t); t += dppf<ROR4>(t); t += dppf<ROR8>(t); t = sum16(t, t); return sum32(t, t); }
__device__ __forceinline__ float max_parity(float t) { t = fmaxf(t, dppf<XOR2>(t)); t = fmaxf(t, dppf<ROR4>(t)); t = fmaxf(t, dppf<ROR8>(t)); return max32(max16(t)); }
__device__ __forceinline__ void ln_stats(u32x4 v, float& mean, float& rden) {
  const float piv = lo16(rfl(v.x));
  f32x2 d[4];
#pragma unroll
  for (int i = 0; i < 4; ++i) d[i] = f32x2{lo16(v[i]), hi16(v[i])} - piv;
  const f32x2 s2 = (d[0] + d[1]) + (d[2] + d[3]), q2 = __builtin_elementwise_fma(d[0], d[0], d[1] * d[1]) + __builtin_elementwise_fma(d[2], d[2], d[3] * d[3]);
  float t = sum32(s2.x + s2.y, q2.x + q2.y);
  t = sum16(t, t);
  t += dppf<XOR1>(t); t += dppf<XOR2>(t); t += dppf<ROR4>(t); t += dppf<ROR8>(t);
  auto r = __builtin_amdgcn_permlane32_swap(asu(t), asu(t), false, false);
  const float ds = asf(r[0]) * (1.f / D), dq = asf(r[1]) * (1.f / D);
  mean = rbf(piv + ds);
  const float var = rbf(fmaxf(fmaf(-ds, ds, dq), 0.f));
  rden = rcp(rbf(__builtin_amdgcn_sqrtf(rbf(var + 1e-5f))));
}
template <int NR> __device__ __forceinline__ void pair_rows(const u32x4 (&w)[NR], u32x4 (&p)[NR]) {
#pragma unroll
  for (int i = 0; i < NR; i += 2) {
    p[i] = u32x4{plo(w[i + 1].x, w[i].x), phi(w[i + 1].x, w[i].x), plo(w[i + 1].y, w[i].y), phi(w[i + 1].y, w[i].y)};
    p[i + 1] = u32x4{plo(w[i + 1].z, w[i].z), phi(w[i + 1].z, w[i].z), plo(w[i + 1].w, w[i].w), phi(w[i + 1].w, w[i].w)};
    pin(p[i]); pin(p[i + 1]);
  }
}
template <int NR> __device__ __forceinline__ void dots(const u32x4 (&p)[NR], const u32 (&x)[NR / 2], float& w0, float& w1) {
  float v[8];
#pragma unroll
  for (int j = 0; j < 8; ++j) { v[j] = 0.f;
#pragma unroll
    for (int i = 0; i < NR / 2; ++i) v[j] = dot2(p[2 * i + (j >> 2)][j & 3], x[i], v[j]); }
  w0 = sum16(sum32(v[0], v[1]), sum32(v[2], v[3])); w1 = sum16(sum32(v[4], v[5]), sum32(v[6], v[7]));
  w0 += dppf<ROR8>(w0); w1 += dppf<ROR8>(w1);
}
__device__ __forceinline__ float gemv_wide(const u32x4 (&p)[8], const u32 (&x)[4], int lane) { float a, b; dots<8>(p, x, a, b); return (lane & 8) ? b : a; }
__device__ __forceinline__ float gemv_narrow(const u32x4 (&p)[4], const u32 (&x)[2], int lane) {
  float a, b; dots<4>(p, x, a, b); a += dppf<ROR4>(a); b += dppf<ROR4>(b); return (lane & 8) ? b : a;
}
__device__ __forceinline__ u32x4 spread8(u32 v, int lane) {
  const u32 pk = __builtin_amdgcn_perm(dppu<XOR1>(v), v, (lane & 1) ? 0x01000504u : 0x05040100u);
  const u32 a = dppu<QB0>(pk), b = dppu<QB2>(pk);
  return u32x4{a, b, dppu<HMIR>(a), dppu<HMIR>(b)};
}
__device__ __forceinline__ void frag2(u32x4 a, u32x4 b, u32x4& c0, u32x4& c1) {
  c0 = u32x4{plo(a.y, a.x), plo(a.w, a.z), plo(b.y, b.x), plo(b.w, b.z)};
  c1 = u32x4{phi(a.y, a.x), phi(a.w, a.z), phi(b.y, b.x), phi(b.w, b.z)};
  pin(c0); pin(c1);
}
__device__ __forceinline__ void frag4(u32x4 a, u32x4 b, u32x4 c, u32x4 d, u32x4 (&f)[4]) {
  frag2(u32x4{a.x, a.z, b.x, b.z}, u32x4{c.x, c.z, d.x, d.z}, f[0], f[1]);
  frag2(u32x4{a.y, a.w, b.y, b.w}, u32x4{c.y, c.w, d.y, d.w}, f[2], f[3]);
}

__global__ void __launch_bounds__(NTHR, 3)
decode_block(const u16* __restrict__ xin, const u16* __restrict__ kc, const u16* __restrict__ vc, const u16* __restrict__ wq,
             const u16* __restrict__ wo, const u16* __restrict__ wg, const u16* __restrict__ wu, const u16* __restrict__ wd,
             const u16* __restrict__ cin, const u16* __restrict__ wmod, u16* __restrict__ out, unsigned char* ws) {
  u32 junk = 0;
  if (threadIdx.x < CODE / 64) {
    u64 pc;
    asm volatile("s_getpc_b64 %1\n\tglobal_load_dword %0, %2, %1" : "+v"(junk), "=&s"(pc) : "v"((blockIdx.x >> 3) * CODE + threadIdx.x * 64));
  }
  __shared__ __attribute__((aligned(16))) unsigned char lds_raw[L_END];
  LAS unsigned char* L = (LAS unsigned char*)lds_raw;
  volatile LAS u32* ctl = (volatile LAS u32*)(L + L_CTL);
  const int lane = threadIdx.x & 63, wave = (int)rfl(threadIdx.x >> 6);
  const int b = blockIdx.x, x = b & 7, s = b >> 3, role = s >> 3, ri = s & 7;
  const int rid = 8 * x + ri;
  const int fid = 14 * (x >> 1) + 8 * (x & 1) + ri;
  const int mtask = 4 * x + (ri & 3);
  const bool isF = role == 3 && ri < 8 - 2 * (x & 1);
  const u64 kv_off = (u64)(x * 32 + s) * CHUNK;
  const auto wsr = __builtin_amdgcn_make_buffer_rsrc(ws, 0, WS_END, 0x00020000);
#define WS_LD4(off) __builtin_amdgcn_raw_buffer_load_b128(wsr, (off), 0, 16)
#define WS_LD1(off) __builtin_amdgcn_raw_buffer_load_b32(wsr, (off), 0, 16)
#define WS_ST4(v, off) __builtin_amdgcn_raw_buffer_store_b128((v), wsr, (off), 0, 16)
#define WS_ST2(v, off) __builtin_amdgcn_raw_buffer_store_b64((v), wsr, (off), 0, 16)
#define WS_ST1(v, off) __builtin_amdgcn_raw_buffer_store_b32((v), wsr, (off), 0, 16)
#define WS_G(off) ((GAS u64*)(ws + (off)))
  auto wait_lds = [&](int word, u32 need) {
    SPIN(n, SPIN_LDS) { if (rfl(ctl[word]) >= need) break; __builtin_amdgcn_s_sleep(1); }
    CBAR();
  };

  if (wave == 0) {
    const u32 t0 = now32();
    ctl[C_BASE] = t0;
    const u64 ka = (u64)kc + kv_off, va = (u64)vc + kv_off;
    const u32 lbase = (u32)(uintptr_t)L + L_RING;
    const int r = lane >> 4, pos = lane & 15;
    LGKM0();
    __builtin_amdgcn_s_barrier();
    __builtin_amdgcn_s_setprio(3);
    asm volatile("s_waitcnt vmcnt(0)" : "+v"(junk));
    u32 tnext = (u32)T_START << 16, slot = 0, pub = 0, now = 0, room = NS, known = 0;
    auto freed = [&]() {
      u32 d = ctl[C_DONE + (lane & 7)];
      d = min(d, dppu<XOR1>(d)); d = min(d, dppu<XOR2>(d)); d = min(d, dppu<HMIR>(d));
      return rfl(d);
    };
#pragma unroll 1
    for (int ph = 0; ph < 2; ++ph) {
      const u64 a = ph ? va : ka;
      const u32x4 rs = {(u32)a, (u32)(a >> 32) & 0xffffu, (u32)CHUNK, 0x00020000u};
      const int sw = ph ? 0 : 15;
      const u32 v0 = r * 256 + ((pos ^ (r & sw)) << 4), v1 = r * 256 + ((pos ^ ((4 + r) & sw)) << 4),
                v2 = r * 256 + ((pos ^ ((8 + r) & sw)) << 4), v3 = r * 256 + ((pos ^ ((12 + r) & sw)) << 4);
      const int tend = ph ? NTILE - ND : NTILE;
      int skip = ph ? NTILE - 8 * (ND - 1) - 1 : -1;
      u32 goff = 0;
#pragma unroll 1
      for (int t = 0; t < tend; t += G) {
        const u32 tile = ph * NTILE + t;
        if (!known && (known = rfl(ctl[C_SKEW]))) tnext += (u32)max(stream_shift((int)known - SKEW0), -(int)(tnext >> 16)) << 16;
        const bool late = now >= (tnext >> 16);
        while (now < (tnext >> 16)) now = now32() - t0;
        tnext += tile < SLOW_N ? TSTEP + TSLOW : TSTEP;
        if (tile + G > room) {
          room = freed() + NS;
          if (tile + G > room) {
            VM0(); pub = tile; ctl[C_LANDED] = pub;
            SPIN(n, SPIN_LDS) { __builtin_amdgcn_s_sleep(1); room = freed() + NS; if (tile + G <= room) break; }
          }
        }
        u32 lag;
        if (late) { asm volatile("s_waitcnt vmcnt(%0)" ::"n"(WMAX - 4 * G) : "memory"); lag = WMAX / 4 - G; }
        else { asm volatile("s_waitcnt vmcnt(%0)" ::"n"(WPUB) : "memory"); lag = WPUB / 4; }
        if (tile > pub + lag) { pub = tile - lag; ctl[C_LANDED] = pub; }
#pragma unroll
        for (int i = 0; i < G; ++i) {
          asm volatile("s_mov_b32 m0, %0\n\ts_nop 0\n\t"
                       "buffer_load_dwordx4 %1, %5, %6 offen nt lds\n\t"
                       "buffer_load_dwordx4 %2, %5, %6 offen offset:1024 nt lds\n\t"
                       "buffer_load_dwordx4 %3, %5, %6 offen offset:2048 nt lds\n\t"
                       "buffer_load_dwordx4 %4, %5, %6 offen offset:3072 nt lds"
                       ::"s"(lbase + (slot + i) * 4096), "v"(v0), "v"(v1), "v"(v2), "v"(v3), "s"(rs),
                         "s"(goff) : "memory");
          const bool jump = t + i + 1 == skip;
          goff = rfl(goff + (jump ? 8192 : 4096)); skip += jump ? 7 : 0;
        }
        slot += G; if (slot >= NS) slot = 0;
      }
    }
#pragma unroll
    for (int k = WMAX / 4 - 1; k >= 0; --k) {
      asm volatile("s_waitcnt vmcnt(%0)" ::"n"(4 * k) : "memory");
      if (2 * NTILE - ND - k > pub) ctl[C_LANDED] = 2 * NTILE - ND - k;
    }
    return;
  }

  if (wave == 1) {
    if (lane < C_N) ctl[lane] = lane < NCW ? lane : 0;
    LGKM0();
    __builtin_amdgcn_s_barrier();
    SBAR();
    const u32 t0 = rfl(ctl[C_BASE]);
    const u64 ep = __hip_atomic_load(WS_G(WS_EPOCH), RLX_AGENT);
    const int j8 = 4 * (lane & 1) + (lane >> 5) + 2 * ((lane >> 4) & 1);
    const u32 xraw = *(const GAS u16*)(xin + 8 * rid + j8);
    const u32x4 ones = {~0u, ~0u, ~0u, ~0u};
    if (b == 0 && lane == 0) WS_ST1(0u, WS_LATE);
    if (lane < 2) WS_ST4(ones, WS_ML + x * 1024 + s * 32 + lane * 16);
    if (role == 0) {
      const u32x4 tail = lane < 48 ? ones : u32x4{0u, 0u, 0u, 0u};
      if (ri >= 4 && lane < 8) WS_ST4(ones, WS_MOD + mtask * 128 + lane * 16);
      WS_ST4(ones, WS_M2 + rid * 2048 + lane * 16); WS_ST4(ones, WS_M2 + rid * 2048 + 1024 + lane * 16);
      WS_ST4(ones, WS_M4 + rid * 2048 + lane * 16); WS_ST4(tail, WS_M4 + rid * 2048 + 1024 + lane * 16);
    } else if (role == 2) {
#pragma unroll
      for (int i = 0; i < 8; ++i) WS_ST4(ones, WS_O + rid * 8192 + i * 1024 + lane * 16);
    } else if (isF) {
      WS_ST4(ones, WS_X1 + fid * 1024 + lane * 16);
    }
    SBAR();
    const u64 tag = ep + 1;
    if (s == 0 && lane == 0) __hip_atomic_store(WS_G(WS_T0 + x * 128), (tag << 32) | t0, RLX_AGENT);
    VM0();
    if (lane == 0) __hip_atomic_store(WS_G(WS_READY + b * 8), tag, RLX_AGENT);
    int skew;
    {
      int d;
      for (u32 t = T_T0;; t += 60) {
        until(t0 + t);
        const u64 e = __hip_atomic_load(WS_G(WS_T0 + (lane & 7) * 128), RLX_AGENT);
        const bool ok = (u32)(e >> 32) == (u32)tag;
        d = ok ? (int)((u32)e - t0) : -1000;
        if (__all(ok) || t > T_T0 + 200) break;
      }
      int e = d;
      d = max(d, (int)dppu<XOR1>(d)); d = max(d, (int)dppu<XOR2>(d)); d = max(d, (int)dppu<HMIR>(d));
      e = min(e, (int)dppu<XOR1>(e)); e = min(e, (int)dppu<XOR2>(e)); e = min(e, (int)dppu<HMIR>(e));
      skew = (int)rfl(d - min((d - e) >> 1, T_CATCH));
      skew = skew < -300 || skew > 300 ? 0 : skew;
      ctl[C_SKEW] = skew + SKEW0;
    }
    const u32 tail0 = t0 + skew + T_LAST;
    if (role == 0 && ri >= 4) {
      wait_lds(C_MODRDY, 1);
      const f32x2 mv = *(const LAS f32x2*)(L + L_MODV + (lane & 31) * 8);
      if (lane < 32) WS_ST1(canon(pk2(mv.x, mv.y)), WS_MOD + mtask * 128 + lane * 4);
    }
    {
      const u32 thi = (u32)(tag >> 16), tlo = (u32)tag & 0xffffu;
      u64 g[8];
      bool rdy;
      while (now32() - t0 < (u32)(T_Q + skew)) __builtin_amdgcn_s_sleep(2);
      SPIN(n, SPIN_MEM) {
        bool ok = true;
#pragma unroll
        for (int i = 0; i < 8; ++i) g[i] = __hip_atomic_load(WS_G(WS_Q + x * 4096 + i * 512 + lane * 8), RLX_AGENT);
        rdy = true;
#pragma unroll
        for (int i = 0; i < 4; ++i) rdy &= __hip_atomic_load(WS_G(WS_READY + (i * 64 + lane) * 8), RLX_AGENT) == tag;
#pragma unroll
        for (int i = 0; i < 8; ++i) ok &= (u32)(g[i] >> 32) == thi && ((u32)g[i] >> 16) == tlo;
        if (__all(ok && rdy)) break;
        __builtin_amdgcn_s_sleep(RETRY);
      }
#pragma unroll
      for (int i = 0; i < 8; ++i) *(LAS u16*)(L + L_QB + i * 128 + lane * 2) = (u16)g[i];
      CBAR();
      ctl[C_QRDY] = 1;
    }
    float ga, gb;
    {
      SPIN(n, SPIN_LDS) { if (rfl(ctl[C_MLCNT]) >= NCW) break; __builtin_amdgcn_s_sleep(8); }
      CBAR();
      const f32x2 ml = *(const LAS f32x2*)(L + L_ML + (lane & 31) * 8);
      float m = fmaxf(ml.x, dppf<ROR4>(ml.x)); m = fmaxf(m, dppf<ROR8>(m)); m = max16(m);
      float l = ml.y * ex2((ml.x - m) * LOG2E);
      l += dppf<ROR4>(l); l += dppf<ROR8>(l); l = sum16(l, l);
      const u32x2 v = {canon(asu(m)), canon(asu(l))};
      if (lane < 4) WS_ST2(v, WS_ML + x * 1024 + s * 32 + lane * 8);
      const u32 t1 = now32();
      while (now32() - t1 < T_ML) __builtin_amdgcn_s_sleep(16);
      u32x4 g;
      u32 g0 = 0, g1 = 0;
      SPIN(n, SPIN_MEM) {
        CBAR();
        g = WS_LD4(WS_ML + x * 1024 + lane * 16);
        if (role == 0) { g0 = WS_LD1(WS_MOD + rid * 16 + (j8 >> 1) * 4); g1 = WS_LD1(WS_MOD + 3072 + rid * 16 + (j8 >> 1) * 4); }
        if (!__any(bad4(g) || max(g0, g1) == 0xffffffffu)) break;
        __builtin_amdgcn_s_sleep(RETRY);
      }
      ga = (j8 & 1) ? hi16(g0) : lo16(g0); gb = (j8 & 1) ? hi16(g1) : lo16(g1);
      const float ma = max_parity(asf(g.x)), mb = max_parity(asf(g.z));
      const float la = sum_parity(asf(g.y) * ex2((asf(g.x) - ma) * LOG2E)), lb = sum_parity(asf(g.w) * ex2((asf(g.z) - mb) * LOG2E));
      if (lane < 2) *(LAS f32x4*)(L + L_FF + lane * 16) = f32x4{ma, 1.f / la, mb, 1.f / lb};
      CBAR();
      ctl[C_FRDY] = 1;
    }
    if (role != 0) return;
    LGKM0();
    __builtin_amdgcn_s_barrier();
    auto gather64 = [&](u32 off, u32 t) {
      u32x4 g0, g1;
      SPIN(n, SPIN_MEM) {
        until(t); t += T_AGAIN;
        CBAR();
        g0 = WS_LD4(off); g1 = WS_LD4(off + 1024);
        if (!__any(bad4(g0) || bad4(g1))) break;
      }
      return red4_parity(asf(g0.x) + asf(g1.x), asf(g0.y) + asf(g1.y), asf(g0.z) + asf(g1.z), asf(g0.w) + asf(g1.w));
    };
    u32 o2 = WS_M2 + rid * 2048 + lane * 16, o4 = WS_M4 + rid * 2048 + lane * 16, ox = WS_X1 + lane * 1024 + rid * 16, oo = (8 * rid + j8) * 2;
    pin(o2); pin(o4); pin(ox); pin(oo);
    float xres = lo16(xraw);
    asm volatile("" : "+v"(ga), "+v"(gb), "+v"(xres));
    until(tail0 + T_B - 40);
    CBAR();
    const u32 late = min(rfl(WS_LD1(WS_LATE)), 400u);
    const float att = gather64(o2, tail0 + late + T_B);
    const float x1 = rbf(xres + rbf(ga * rbf(att)));
    {
      auto t = __builtin_amdgcn_permlane32_swap(asu(x1), asu(x1), false, false);
      const u32 pk = canon(phi(t[1], t[0]));
      auto u = __builtin_amdgcn_permlane16_swap(pk, pk, false, false);
      const u32 n0 = dppu<XOR1>(u[0]), n1 = dppu<XOR1>(u[1]);
      const u32x4 v = (lane & 1) ? u32x4{n0, n1, u[0], u[1]} : u32x4{u[0], u[1], n0, n1};
      if (lane < NF) WS_ST4(v, ox);
    }
    if (b == 0 && lane == 0) __hip_atomic_store(WS_G(WS_EPOCH), tag, RLX_AGENT);
    const float y = gather64(o4, tail0 + late + T_D);
    if ((lane & 14) == 0) *(GAS u16*)((GAS unsigned char*)out + oo) = (u16)bfbits(x1 + rbf(gb * rbf(y)));
    return;
  }

  const int w = wave - 2, g = lane >> 4, i16 = lane & 15, a8 = lane >> 3, ch = lane & 7, b2 = (lane >> 2) & 1;
  const int jcol = (lane >> 5) + 2 * ((lane >> 4) & 1) + 4 * ((lane >> 3) & 1);
  LAS float* red = (LAS float*)(L + L_RED);
  auto last_arriver = [&]() {
    CBAR();
    u32 old = 0;
    if (lane == 0) old = __hip_atomic_fetch_add((LAS u32*)(L + L_CTL) + C_CNT1, 1u, RLX_WG);
    const bool last = rfl(old) == NCW - 1;
    CBAR();
    return last;
  };
  u32x4 wp[8];
  u32x4 wu0[2];
  u64 tag = 0;
  float lnx = 0.f;
  const int r4 = 64 * w + 4 * (lane >> 2), qtask = 16 * x + (role == 3 ? 8 : 0) + ri;
  if (role == 2) {
    SBAR();
    __builtin_amdgcn_s_barrier();
    SBAR();
#pragma unroll
    for (int i = 0; i < 8; ++i) {
      const u16* p = wo + (size_t)(64 * rid + 32 * (i >> 2) + 8 * g + 2 * (i & 3)) * D + 64 * w + 4 * i16;
      const u32x2 r0 = ldnt<u32x2>(p), r1 = ldnt<u32x2>(p + D);
      wp[i] = u32x4{r0.x, r0.y, r1.x, r1.y};
    }
  } else if (role == 0 && ri >= 4) {
    u32x4 wn[8];
    SBAR();
    __builtin_amdgcn_s_barrier();
    SBAR();
#pragma unroll
    for (int i = 0; i < 8; ++i) wn[i] = ldnt<u32x4>(wmod + (size_t)(64 * w + 8 * a8 + i) * (6 * D) + 2 * D + 64 * mtask + 8 * ch);
    SBAR();
    const u32x4 craw = *(const GAS u32x4*)(cin + 64 * w + 8 * a8);
    SBAR();
    u32 xs[4];
#pragma unroll
    for (int i = 0; i < 4; ++i) xs[i] = pk2(silu(lo16(craw[i])), silu(hi16(craw[i])));
    pair_rows<8>(wn, wp);
    red[w * 64 + 8 * ch + jcol] = gemv_wide(wp, xs, lane);
    if (last_arriver()) {
      float t = sum8(red + lane);
      t = rbf(t);
      if ((mtask >> 3) == 2) t = rbf(1.f + t);
      ((LAS float*)(L + L_MODV))[lane] = t;
      CBAR();
      ctl[C_MODRDY] = 1;
    }
  } else if (role != 0) {
    u32x4 wn[4];
    const u16* p0 = wq + (size_t)r4 * NQ + 32 * qtask + 8 * (lane & 3);
    wn[0] = ldnt<u32x4>(p0); wn[1] = ldnt<u32x4>(p0 + NQ);
    SBAR();
    __builtin_amdgcn_s_barrier();
    SBAR();
    wn[2] = ldnt<u32x4>(p0 + 2 * NQ); wn[3] = ldnt<u32x4>(p0 + 3 * NQ);
    SBAR();
    const u32x4 xv = *(const GAS u32x4*)(xin + lane * 8);
    const u32 xraw = *(const GAS u16*)(xin + 64 * w + lane);
    const u64 ep = __hip_atomic_load(WS_G(WS_EPOCH), RLX_AGENT);
    SBAR();
    if (isF) {
      const u16* p = wg + (size_t)(64 * w + 8 * g) * FF + 32 * fid + 2 * i16;
#pragma unroll
      for (int i = 0; i < 4; ++i) {
        const u16* q = p + (32 * (i >> 1) + 4 * (i & 1)) * FF;
        wp[4 + i] = u32x4{ldnt<u32>(q), ldnt<u32>(q + FF), ldnt<u32>(q + 2 * FF), ldnt<u32>(q + 3 * FF)};
      }
#pragma unroll
      for (int i = 0; i < 2; ++i) {
        const u16* q = wu + (p - wg) + 4 * i * FF;
        wu0[i] = u32x4{ldnt<u32>(q), ldnt<u32>(q + FF), ldnt<u32>(q + 2 * FF), ldnt<u32>(q + 3 * FF)};
      }
    }
    SBAR();
    float mean, rden;
    ln_stats(xv, mean, rden);
    lnx = rbf(rbf(lo16(xraw) - mean) * rden);
    asm volatile("" : "+v"(lnx));
    tag = (((u64)rfl((u32)(ep >> 32)) << 32) | rfl((u32)ep)) + 1;
    pair_rows<4>(wn, (u32x4 (&)[4])wp[0]);
  } else {
    u32x4 wn[4], wp2[4];
    const u16* p = wmod + (size_t)r4 * (6 * D) + 32 * mtask + 8 * (lane & 3);
    wn[0] = ldnt<u32x4>(p); wn[1] = ldnt<u32x4>(p + 6 * D);
    SBAR();
    __builtin_amdgcn_s_barrier();
    SBAR();
    wn[2] = ldnt<u32x4>(p + 12 * D); wn[3] = ldnt<u32x4>(p + 18 * D);
    SBAR();
    const u32x2 craw = *(const GAS u32x2*)(cin + r4);
    const u64 ep = __hip_atomic_load(WS_G(WS_EPOCH), RLX_AGENT);
    SBAR();
    const u32 xs[2] = {pk2(silu(lo16(craw.x)), silu(hi16(craw.x))), pk2(silu(lo16(craw.y)), silu(hi16(craw.y)))};
    pair_rows<4>(wn, wp2);
    red[w * 64 + 8 * (lane & 3) + jcol] = gemv_narrow(wp2, xs, lane);
    if (last_arriver()) {
      float t = sum8(red + (lane & 31));
      t = rbf(t);
      if (mtask >= 16) t = rbf(1.f + t);
      if (lane < 32) __hip_atomic_store(WS_G(WS_H + (32 * mtask + lane) * 8), ((ep + 1) << 16) | (asu(t) >> 16), RLX_AGENT);
    }
  }
  const u32 tw0 = rfl(ctl[C_BASE]);
  auto q_task = [&]() {
    u64 sg, cg;
    wait_lds(C_SKEW, 1);
    const u32 th = T_H + rfl(ctl[C_SKEW]) - SKEW0;
    while (now32() - tw0 < th) __builtin_amdgcn_s_sleep(1);
    SPIN(n, SPIN_MEM) {
      sg = __hip_atomic_load(WS_G(WS_H + (64 * w + lane) * 8), RLX_AGENT);
      cg = __hip_atomic_load(WS_G(WS_H + 4096 + (64 * w + lane) * 8), RLX_AGENT);
      if (__all((sg >> 16) == (tag & 0xffffffffffffull) && (cg >> 16) == (tag & 0xffffffffffffull))) break;
      __builtin_amdgcn_s_sleep(RETRY / 2);
    }
    const u32 h = pk2(rbf(lnx * lo16((u32)cg)) + lo16((u32)sg), 0.f);
    const u32 pk = __builtin_amdgcn_perm(dppu<XOR1>(h), h, (lane & 1) ? 0x01000504u : 0x05040100u);
    const u32 xs[2] = {dppu<QB0>(pk), dppu<QB2>(pk)};
    red[w * 64 + 8 * (lane & 3) + jcol] = gemv_narrow((const u32x4 (&)[4])wp[0], xs, lane);
    if (last_arriver()) {
      float t = sum8(red + (lane & 31));
      if (lane < 32) __hip_atomic_store(WS_G(WS_Q + (32 * qtask + lane) * 8), (tag << 16) | bfbits(t), RLX_AGENT);
    }
  };

  const int head = i16 >> 2;
  LAS unsigned char* sc = L + L_SC + w * 2048;
  u32 slot = w;
  auto fetch = [&](u32 pos, u32 o0, u32 o1, u32 o2, u32 o3, u32x4 (&f)[4]) {
    wait_lds(C_LANDED, pos + 1);
    const LAS unsigned char* p = L + L_RING + slot * 4096;
    f[0] = *(const LAS u32x4*)(p + o0); f[1] = *(const LAS u32x4*)(p + o1); f[2] = *(const LAS u32x4*)(p + o2); f[3] = *(const LAS u32x4*)(p + o3);
    SBAR();
    asm volatile("s_waitcnt lgkmcnt(0)" : "+v"(f[0]), "+v"(f[1]), "+v"(f[2]), "+v"(f[3]) :: "memory");
    ctl[C_DONE + w] = pos + NCW;
    slot += NCW; if (slot >= NS) slot -= NS;
  };
  float m_w;
  {
    const u32 ko = i16 * 256 + ((g ^ i16) << 4);
    static_assert(QAT >= 1 && QAT <= PRE, "the Q task runs between two parked tiles");
    u32x4 pre[PRE][4];
#pragma unroll
    for (int i = 0; i < PRE; ++i) {
      fetch(i * NCW + w, ko, ko ^ 64, ko ^ 128, ko ^ 192, pre[i]);
      if (i == QAT - 1 && (role & 1)) { q_task(); wp[0] = wu0[0]; wp[1] = wu0[1]; pin(wp[0]); pin(wp[1]); }
    }
    wait_lds(C_QRDY, 1);
    u32x4 qf[4];
#pragma unroll
    for (int ks = 0; ks < 4; ++ks) qf[ks] = *(const LAS u32x4*)(L + L_QB + head * 256 + ks * 64 + g * 16);
    float mx = -INFINITY;
    auto score = [&](int i, const u32x4 (&kf)[4]) {
      f32x4 d = {0.f, 0.f, 0.f, 0.f};
#pragma unroll
      for (int ks = 0; ks < 4; ++ks) d = mfma32(kf[ks], qf[ks], d);
      mx = fmaxf(fmaxf(mx, fmaxf(d.x, d.y)), fmaxf(d.z, d.w));
      const u32 r0 = pk2(d.x, d.y), r1 = pk2(d.z, d.w);
      const u32x2 z = {pk2(lo16(r0) * SCALE, hi16(r0) * SCALE), pk2(lo16(r1) * SCALE, hi16(r1) * SCALE)};
      if ((lane & 3) == 0) *(LAS u32x2*)(sc + i * 128 + g * 32 + head * 8) = z;
    };
#pragma unroll
    for (int i = 0; i < PRE; ++i) score(i, pre[i]);
#pragma unroll 1
    for (int i = PRE; i < NIT; ++i) {
      u32x4 kf[4];
      fetch(i * NCW + w, ko, ko ^ 64, ko ^ 128, ko ^ 192, kf);
      score(i, kf);
    }
    m_w = rbf(rbf(max32(max16(mx))) * SCALE);
  }
  __builtin_amdgcn_s_waitcnt(0x0F70);
  {
    const u32 mu = asu(m_w);
    const float m0 = asf(__builtin_amdgcn_readlane(mu, 0)), m1 = asf(__builtin_amdgcn_readlane(mu, 4)),
                m2 = asf(__builtin_amdgcn_readlane(mu, 8)), m3 = asf(__builtin_amdgcn_readlane(mu, 12));
    const float ma = (lane & 1) ? m2 : m0, mb = (lane & 1) ? m3 : m1;
    float la = 0.f, lb = 0.f;
    CBAR();
#pragma unroll
    for (int k = 0; k < 2; ++k) {
      u32x4 v = *(const LAS u32x4*)(sc + k * 1024 + lane * 16);
#pragma unroll
      for (int d = 0; d < 4; ++d) {
        const float mm = d < 2 ? ma : mb;
        const float p0 = ex2((lo16(v[d]) - mm) * LOG2E), p1 = ex2((hi16(v[d]) - mm) * LOG2E);
        if (d < 2) la += p0 + p1; else lb += p0 + p1;
        v[d] = pk2(p0, p1);
      }
      *(LAS u32x4*)(sc + k * 1024 + lane * 16) = v;
    }
    la = sum_parity(la); lb = sum_parity(lb);
    if (lane < 2) *(LAS f32x4*)(L + L_ML + w * 32 + lane * 16) = f32x4{ma, la, mb, lb};
    CBAR();
    if (lane == 0) __hip_atomic_fetch_add((LAS u32*)(L + L_CTL) + C_MLCNT, 1u, RLX_WG);
    m_w = g == 0 ? m0 : g == 1 ? m1 : g == 2 ? m2 : m3;
  }
  f32x4 acc0 = {0.f, 0.f, 0.f, 0.f}, acc1 = {0.f, 0.f, 0.f, 0.f};
  const int js = lane & 3;
  const u32 sx0 = js == 0 ? 0x0c0c0100u : js == 1 ? 0x01000c0cu : 0x0c0c0c0cu, sx1 = js == 0 ? 0x0c0c0302u : js == 1 ? 0x03020c0cu : 0x0c0c0c0cu;
  const u32 sy0 = js == 2 ? 0x0c0c0100u : js == 3 ? 0x01000c0cu : 0x0c0c0c0cu, sy1 = js == 2 ? 0x0c0c0302u : js == 3 ? 0x03020c0cu : 0x0c0c0c0cu;
  const u32 vo = g * 1024 + i16 * 16;
  auto p_frags = [&](int i, u32x2 (&pa)[4]) {
    const u32x2 p = *(const LAS u32x2*)(sc + i * 128 + g * 32 + head * 8);
#pragma unroll
    for (int t = 0; t < 4; ++t) pa[t] = u32x2{__builtin_amdgcn_perm(0u, p[t >> 1], (t & 1) ? sx1 : sx0), __builtin_amdgcn_perm(0u, p[t >> 1], (t & 1) ? sy1 : sy0)};
  };
  auto pv = [&](const u32x2 (&pa)[4], const u32x4 (&vf)[4]) {
#pragma unroll
    for (int t = 0; t < 4; ++t) { acc0 = mfma16(pa[t], u32x2{vf[t].x, vf[t].y}, acc0); acc1 = mfma16(pa[t], u32x2{vf[t].z, vf[t].w}, acc1); }
  };
  auto vtile = [&](int i, u32 pos) {
    u32x2 pa[4]; u32x4 vf[4];
    p_frags(i, pa);
    fetch(pos, vo, vo + 256, vo + 512, vo + 768, vf);
    pv(pa, vf);
  };
#pragma unroll 1
  for (int i = 0; i < NIT - ND; ++i) vtile(i, NTILE + i * NCW + w);
  wait_lds(C_FRDY, 1);
  const f32x2 ff = *(const LAS f32x2*)(L + L_FF + g * 8);
  float fac = ex2((m_w - ff.x) * LOG2E) * ff.y;
  asm volatile("" : "+v"(fac));
  if (role == 2) {
    u32x4 f0[4], f1[4];
    frag4(wp[0], wp[1], wp[2], wp[3], f0); frag4(wp[4], wp[5], wp[6], wp[7], f1);
#pragma unroll
    for (int j = 0; j < 4; ++j) { wp[j] = f0[j]; wp[4 + j] = f1[j]; }
  } else if (isF) {
#pragma unroll
    for (int j = 4; j < 8; j += 2) { u32x4 c0, c1; frag2(wp[j], wp[j + 1], c0, c1); wp[j] = c0; wp[j + 1] = c1; }
    { u32x4 c0, c1; frag2(wp[0], wp[1], c0, c1); wp[0] = c0; wp[1] = c1; }
  }
  const int skew = (int)rfl(ctl[C_SKEW]) - SKEW0;
  const u32 tailw = tw0 + skew + T_LAST;
  const int frow = 64 * w + 32 * (a8 & 1) + 8 * g + ch;
  u32 c1v = 0, shv = 0;
  if (isF) {
    SPIN(n, SPIN_MEM) {
      CBAR();
      c1v = WS_LD1(WS_MOD + 2048 + (frow >> 1) * 4); shv = WS_LD1(WS_MOD + 1024 + (frow >> 1) * 4);
      if (!__any(max(c1v, shv) == 0xffffffffu)) break;
      __builtin_amdgcn_s_sleep(RETRY);
    }
  }
  if (w < NCW - 1) {
#pragma unroll 1
    for (int i = NIT - ND; i < NIT; ++i) {
      vtile(i, NTILE + i * NCW + w - (i - (NIT - ND)));
      slot = slot ? slot - 1 : NS - 1;
    }
    *(LAS f32x4*)(sc + g * 512 + i16 * 32) = acc0 * fac;
    *(LAS f32x4*)(sc + g * 512 + i16 * 32 + 16) = acc1 * fac;
    CBAR();
    if (lane == 0) __hip_atomic_fetch_add((LAS u32*)(L + L_CTL) + C_MRG, 1u, RLX_WG);
  } else {
    u32 off = WS_O + (8 * x + 2 * g + (i16 >> 3)) * 8192 + ch * 1024 + s * 32;
    pin(off);
    u32x2 pa[2][4];
    u32x4 va[ND][4];
    const unsigned char* vp = (const unsigned char*)vc + kv_off + vo;

#pragma unroll
    for (int k = 0; k < ND; ++k) {
      if (k % G == 0) until(tw0 + stream_shift(skew) + t_group((2 * NTILE - ND + k) / G));
#pragma unroll
      for (int t = 0; t < 4; ++t) va[k][t] = ldnt<u32x4>(vp + (NTILE - 1 - 8 * (ND - 1 - k)) * 4096 + t * 256);
      SBAR();
    }
#pragma unroll
    for (int k = 0; k < ND - 2; ++k) { p_frags(NIT - ND + k, pa[0]); pv(pa[0], va[k]); }
    p_frags(NIT - 2, pa[0]); p_frags(NIT - 1, pa[1]);
    asm volatile("" : "+v"(pa[0][0]), "+v"(pa[0][1]), "+v"(pa[0][2]), "+v"(pa[0][3]), "+v"(pa[1][0]), "+v"(pa[1][1]), "+v"(pa[1][2]), "+v"(pa[1][3]), "+v"(acc0), "+v"(acc1));
    SBAR();
    SPIN(n, SPIN_LDS) { if (rfl(ctl[C_MRG]) >= NCW - 1) break; }
    CBAR();
    f32x4 o0 = {0.f, 0.f, 0.f, 0.f}, o1 = {0.f, 0.f, 0.f, 0.f};
#pragma unroll
    for (int i = 0; i < NCW - 1; ++i) {
      o0 += *(const LAS f32x4*)(L + L_SC + i * 2048 + g * 512 + i16 * 32); o1 += *(const LAS f32x4*)(L + L_SC + i * 2048 + g * 512 + i16 * 32 + 16);
    }
    asm volatile("" : "+v"(o0), "+v"(o1));
    SBAR();
    pv(pa[0], va[ND - 2]); pv(pa[1], va[ND - 1]);
    o0 += acc0 * fac; o1 += acc1 * fac;
    WS_ST4(canon4(o0.x, o0.y, o0.z, o0.w), off); WS_ST4(canon4(o1.x, o1.y, o1.z, o1.w), off + 16);
    const int d = (int)(now32() - tailw) - T_H1;
    if (d > 0 && lane == 0) __hip_atomic_fetch_max((GAS u32*)(ws + WS_LATE), (u32)d + 25u, RLX_AGENT);
  }

  if (role == 2) {
    u32 oi = WS_O + rid * 8192 + w * 1024 + lane * 16, os = WS_M2 + (8 * w + (i16 >> 1)) * 2048 + rid * 32 + (lane & 1) * 16,
        ol = L_OPK + w * 16 + (4 * (lane & 1) + (lane >> 5) + 2 * ((lane >> 4) & 1)) * 2, or_ = L_OPK + g * 16;
    pin(oi); pin(os); pin(ol); pin(or_);
    __builtin_amdgcn_s_barrier();
    const u32 ta = tailw + T_A;
    u32x4 v;
    SPIN(n, SPIN_MEM) {
      until(ta + n * T_AGAIN);
      CBAR();
      v = WS_LD4(oi);
      if (!__any(bad4(v))) break;
    }
    const float o = red4_parity(asf(v.x), asf(v.y), asf(v.z), asf(v.w));
    if ((lane & 14) == 0) *(LAS u16*)(L + ol) = (u16)bfbits(o);
    LGKM0();
    __builtin_amdgcn_s_barrier();
    CBAR();
    const u32x4 x0 = *(const LAS u32x4*)(L + or_), x1 = *(const LAS u32x4*)(L + or_ + 64);
    f32x4 d[4];
#pragma unroll
    for (int j = 0; j < 4; ++j) d[j] = mfma32(x0, wp[j], f32x4{0.f, 0.f, 0.f, 0.f});
#pragma unroll
    for (int j = 0; j < 4; ++j) d[j] = mfma32(x1, wp[4 + j], d[j]);
    pin4(d[0], d[1], d[2], d[3]);
    if (lane < 16) WS_ST4(canon4(d[0].x, d[1].x, d[2].x, d[3].x), os);
  } else if (isF) {
#define DMA1 "s_mov_b32 m0, %3\n\ts_nop 0\n\tbuffer_load_dwordx4 %1, %0, %2 offen nt lds"
    const u32 lb = L_RING + w * 8192, m = rfl((u32)(uintptr_t)L + lb);
    const u64 ua = (u64)wu + 64 * fid, da = (u64)wd + (u64)fid * 32 * D * 2;
    const u32x4 ru = {(u32)ua, (u32)(ua >> 32) & 0xffffu, D * FF * 2 - 64u * fid, 0x00020000u}, rd = {(u32)da, (u32)(da >> 32) & 0xffffu, 32 * D * 2, 0x00020000u};
    const u32x4 rw = {(u32)(uintptr_t)ws, (u32)((uintptr_t)ws >> 32) & 0xffffu, WS_END, 0x00020000u};
    const u32 vu = (64 * w + 2 * (lane >> 2)) * (FF * 2) + (lane & 3) * 16, vd = a8 * (D * 2) + w * 128 + ch * 16;
    __builtin_amdgcn_s_barrier();
#pragma unroll
    for (int i = 1; i < 4; i += 2) asm volatile(DMA1 ::"s"(ru), "v"(vu), "s"((32 * (i & 1) + (i >> 1)) * FF * 2), "s"(m + i * 1024) : "memory");
#pragma unroll
    for (int i = 0; i < 4; ++i) asm volatile(DMA1 ::"s"(rd), "v"(vd), "s"(i * 8 * D * 2), "s"(m + 4096 + i * 1024) : "memory");
    u32 lraw;
    asm volatile("buffer_load_dword %0, off, %1, 0 offset:%2 sc1" : "=v"(lraw) : "s"(rw), "n"(WS_LATE) : "memory");
    const u32 sh16 = (ch & 1) ? 0u : 16u;
    float c1 = hi16(c1v << sh16), sh = hi16(shv << sh16);
    asm volatile("s_waitcnt vmcnt(5)" : "+v"(c1), "+v"(sh) :: "memory");
#pragma unroll
    for (int ks = 1; ks < 2; ++ks) {
      const LAS u32* e = (const LAS u32*)(L + lb + ks * 1024 + g * 256 + i16 * 4);
      frag2(u32x4{e[0], e[512], e[16], e[528]}, u32x4{e[32], e[544], e[48], e[560]}, wp[2 * ks], wp[2 * ks + 1]);
    }
    u32 oa = WS_X1 + fid * 1024 + lane * 16, ob = WS_X1 + fid * 1024 + (frow >> 3) * 16 + (ch >> 1) * 4;
    u32 os = WS_M4 + (8 * w + (i16 >> 1)) * 2048 + fid * 32 + (lane & 1) * 16, ol = L_RED + w * 256 + lane * 16,
        or_ = L_RED + (16 * g + 4 * (ch >> 1) + (ch & 1) + 2 * (a8 & 1)) * 4;
    pin(oa); pin(ob); pin(os); pin(ol); pin(or_);
    asm volatile("s_waitcnt vmcnt(0)" : "+v"(lraw) :: "memory");
    auto col4 = [&](u32 o) { const LAS u32x2* p = (const LAS u32x2*)(L + o + g * 1024 + i16 * 8); const u32x2 r0 = p[0], r1 = p[16]; return u32x4{r0.x, r0.y, r1.x, r1.y}; };
    u32x4 fd[4];
    frag4(col4(lb + 4096), col4(lb + 4352), col4(lb + 4608), col4(lb + 4864), fd);
    const u32 late = min(rfl(lraw), 400u);
    u32x4 xa; u32 xb;
    SPIN(n, SPIN_MEM) {
      until(tailw + late + T_C + n * T_AGAIN);
      CBAR();
      xa = WS_LD4(oa); xb = WS_LD1(ob);
      if (!__any(bad4(xa) || xb == 0xffffffffu)) break;
    }
    float mean, rden;
    ln_stats(xa, mean, rden);
    const u32x4 x0 = spread8(pk2(rbf(rbf(rbf(hi16(xb << sh16) - mean) * rden) * c1) + sh, 0.f), lane);
    const u32x4 x1 = {dppu<ROR8>(x0.x), dppu<ROR8>(x0.y), dppu<ROR8>(x0.z), dppu<ROR8>(x0.w)};
    const f32x4 z = {0.f, 0.f, 0.f, 0.f};
    f32x4 d0 = mfma32(x0, wp[4], z), d1 = mfma32(x0, wp[5], z), d2 = mfma32(x0, wp[0], z), d3 = mfma32(x0, wp[1], z);
    d0 = mfma32(x1, wp[6], d0); d1 = mfma32(x1, wp[7], d1); d2 = mfma32(x1, wp[2], d2); d3 = mfma32(x1, wp[3], d3);
    pin4(d0, d1, d2, d3);
    if (lane < 16) *(LAS f32x4*)(L + ol) = f32x4{d0.x, d1.x, d2.x, d3.x};
    LGKM0();
    __builtin_amdgcn_s_barrier();
    CBAR();
    const float t = rbf(sum8((const LAS float*)(L + or_)));
    const float o = dppf<ROR8>(t);
    u32x4 act = spread8(bfbits(rbf(silu((a8 & 1) ? o : t)) * ((a8 & 1) ? t : o)), lane);
    f32x4 d[4];
#pragma unroll
    for (int j = 0; j < 4; ++j) d[j] = mfma32(act, fd[j], z);
    pin4(d[0], d[1], d[2], d[3]);
    if (lane < 16) WS_ST4(canon4(d[0].x, d[1].x, d[2].x, d[3].x), os);
  } else if (role == 0) {
    __builtin_amdgcn_s_barrier();
  }
}

extern "C" void kernel_launch(void* const* d_in, const int* in_sizes, int n_in, void* d_out, int out_size, void* d_ws,
                              size_t ws_size, hipStream_t stream) {
  decode_block<<<NWG, NTHR, 0, stream>>>((const u16*)d_in[0], (const u16*)d_in[1], (const u16*)d_in[2], (const u16*)d_in[3],
                                         (const u16*)d_in[4], (const u16*)d_in[6], (const u16*)d_in[7], (const u16*)d_in[8],
                                         (const u16*)d_in[9], (const u16*)d_in[10], (u16*)d_out, (unsigned char*)d_ws);
}
```

```cpp
#include <hip/hip_runtime.h>
#include <hip/hip_bf16.h>
#include <stdint.h>

typedef unsigned short u16;
typedef unsigned u32;
typedef unsigned long long u64;
typedef u32 u32x2 __attribute__((ext_vector_type(2)));
typedef u32 u32x4 __attribute__((ext_vector_type(4)));
typedef float f32x2 __attribute__((ext_vector_type(2)));
typedef float f32x4 __attribute__((ext_vector_type(4)));
typedef short s16x4 __attribute__((ext_vector_type(4)));
typedef __bf16 bf16x2 __attribute__((ext_vector_type(2)));
typedef __bf16 bf16x8 __attribute__((ext_vector_type(8)));
#define GAS __attribute__((address_space(1)))
#define LAS __attribute__((address_space(3)))
#define RLX_AGENT __ATOMIC_RELAXED, __HIP_MEMORY_SCOPE_AGENT
#define RLX_WG __ATOMIC_RELAXED, __HIP_MEMORY_SCOPE_WORKGROUP

constexpr int D = 512, DH = 128, SK = 65536, FF = 1792, NQ = 4096;
constexpr int NWG = 256, NTHR = 640, NCW = 8;
constexpr int CHUNK = (SK / 32) * DH * 2;
constexpr int NTILE = CHUNK / 4096;
constexpr int NIT = NTILE / NCW;
constexpr int NF = 56;
constexpr int G = 2;
constexpr int NS = 34;
constexpr int WPUB = 24;
constexpr int WMAX = 56;
constexpr int PRE = 6;
constexpr int QAT = 2;
constexpr int ND = 4;
constexpr int T_START = 195;
constexpr int T_PACE = 3540;
constexpr int SLOW_N = 40, T_SLOW = 50;
constexpr int T_LEAD = 35;
constexpr int T_CATCH = 15;
constexpr int T_T0 = 250, T_H = 670, T_Q = 1120;
constexpr int T_ML = 600;
constexpr int RETRY = 16;
constexpr int T_H1 = 145, T_A = 170, T_B = T_A + 95, T_C = T_B + 73, T_D = T_C + 137;
constexpr int T_AGAIN = 22;
constexpr int CODE = 660;
static_assert(NS % G == 0 && WPUB % 4 == 0 && WMAX % 4 == 0 && WMAX <= 60 && ND % G == 0 && QAT >= 1 && QAT <= PRE, "geometry");
constexpr u32 TSTEP = (u32)(((u64)T_PACE << 16) / (2 * NTILE / G)), TSLOW = (u32)(((u64)T_SLOW << 16) / (SLOW_N / G));
constexpr u32 t_group(int n) { return T_START + T_SLOW + (u32)(((u64)TSTEP * n) >> 16); }
constexpr u32 T_LAST = t_group(2 * NTILE / G - 1);
constexpr int SKEW0 = 512;
__device__ constexpr int stream_shift(int skew) { return skew > T_LEAD ? skew - T_LEAD : skew < 0 ? skew : 0; }
constexpr u32 SPIN_LDS = 1u << 19, SPIN_MEM = 1u << 17;
constexpr u32 WS_EPOCH = 0, WS_LATE = 1024, WS_T0 = 2048, WS_READY = 4096, WS_H = 8192, WS_Q = WS_H + 8192, WS_ML = WS_Q + 8 * 4096,
              WS_MOD = WS_ML + 8 * 1024, WS_O = WS_MOD + 4096, WS_M2 = WS_O + 64 * 8192, WS_M4 = WS_M2 + 64 * 2048,
              WS_X1 = WS_M4 + 64 * 2048, WS_END = WS_X1 + 64 * 1024;
constexpr int L_CTL = 0, L_OPK = 128, L_FF = L_OPK + 128, L_ML = 512, L_MODV = L_ML + 256, L_QB = 1024, L_RED = L_QB + 1024,
              L_SC = L_RED + 2048, L_RING = L_SC + NCW * 2048, L_END = L_RING + NS * 4096;
static_assert(L_END <= 163840, "LDS budget");
enum { C_DONE = 0, C_LANDED = 8, C_CNT1 = 9, C_MRG = 10, C_QRDY = 11, C_MLCNT = 12, C_FRDY = 13, C_MODRDY = 14, C_SKEW = 16, C_N = 17, C_BASE = 17 };

constexpr float LOG2E = 1.4426950408889634f, SCALE = 0.08838834764831845f;

#define CBAR() asm volatile("" ::: "memory")
#define SBAR() __builtin_amdgcn_sched_barrier(0)
#define LGKM0() asm volatile("s_waitcnt lgkmcnt(0)" ::: "memory")
#define VM0() asm volatile("s_waitcnt vmcnt(0)" ::: "memory")
#define SPIN(n, cap) _Pragma("unroll 1") for (u32 n = 0; n < (cap); ++n)
__device__ __forceinline__ float asf(u32 x) { return __builtin_bit_cast(float, x); }
__device__ __forceinline__ u32 asu(float x) { return __builtin_bit_cast(u32, x); }
__device__ __forceinline__ u32 pk2(float lo, float hi) { f32x2 t = {lo, hi}; return __builtin_bit_cast(u32, __builtin_convertvector(t, bf16x2)); }
__device__ __forceinline__ float lo16(u32 x) { return asf(x << 16); }
__device__ __forceinline__ float hi16(u32 x) { return asf(x & 0xffff0000u); }
__device__ __forceinline__ float rbf(float x) { return lo16(pk2(x, x)); }
__device__ __forceinline__ u32 bfbits(float x) { return pk2(x, x) & 0xffffu; }
__device__ __forceinline__ u32 canon(u32 x) { return x < 0xfffffffeu ? x : 0xfffffffeu; }
__device__ __forceinline__ u32x4 canon4(float a, float b, float c, float d) { return u32x4{canon(asu(a)), canon(asu(b)), canon(asu(c)), canon(asu(d))}; }
__device__ __forceinline__ float ex2(float x) { return __builtin_amdgcn_exp2f(x); }
__device__ __forceinline__ float rcp(float x) { return __builtin_amdgcn_rcpf(x); }
__device__ __forceinline__ float silu(float x) { return x * rcp(1.f + ex2(-LOG2E * x)); }
__device__ __forceinline__ u32 now32() { return (u32)__builtin_amdgcn_s_memrealtime(); }
__device__ __forceinline__ void until(u32 t) { while ((u32)(t - now32() - 1) < 8192u) ; }
template <int CTRL> __device__ __forceinline__ u32 dppu(u32 x) { return (u32)__builtin_amdgcn_mov_dpp((int)x, CTRL, 0xf, 0xf, true); }
template <int CTRL> __device__ __forceinline__ float dppf(float x) { return asf(dppu<CTRL>(asu(x))); }
constexpr int XOR1 = 0xB1, XOR2 = 0x4E, QB0 = 0x00, QB2 = 0xAA, HMIR = 0x141, ROR4 = 0x124, ROR8 = 0x128;
__device__ __forceinline__ float sum32(float a, float b) { auto r = __builtin_amdgcn_permlane32_swap(asu(a), asu(b), false, false); return asf(r[0]) + asf(r[1]); }
__device__ __forceinline__ float sum16(float a, float b) { auto r = __builtin_amdgcn_permlane16_swap(asu(a), asu(b), false, false); return asf(r[0]) + asf(r[1]); }
__device__ __forceinline__ float max32(float a) { auto r = __builtin_amdgcn_permlane32_swap(asu(a), asu(a), false, false); return fmaxf(asf(r[0]), asf(r[1])); }
__device__ __forceinline__ float max16(float a) { auto r = __builtin_amdgcn_permlane16_swap(asu(a), asu(a), false, false); return fmaxf(asf(r[0]), asf(r[1])); }
__device__ __forceinline__ float dot2(u32 w, u32 x, float acc) { return __builtin_amdgcn_fdot2_f32_bf16(__builtin_bit_cast(bf16x2, w), __builtin_bit_cast(bf16x2, x), acc, false); }
__device__ __forceinline__ f32x4 mfma32(u32x4 a, u32x4 b, f32x4 c) { return __builtin_amdgcn_mfma_f32_16x16x32_bf16(__builtin_bit_cast(bf16x8, a), __builtin_bit_cast(bf16x8, b), c, 0, 0, 0); }
__device__ __forceinline__ f32x4 mfma16(u32x2 a, u32x2 b, f32x4 c) { return __builtin_amdgcn_mfma_f32_16x16x16bf16_1k(__builtin_bit_cast(s16x4, a), __builtin_bit_cast(s16x4, b), c, 0, 0, 0); }
__device__ __forceinline__ u32 rfl(u32 x) { return (u32)__builtin_amdgcn_readfirstlane((int)x); }
template <class T> __device__ __forceinline__ T ldnt(const void* p) { return __builtin_nontemporal_load((const GAS T*)p); }
__device__ __forceinline__ bool bad4(u32x4 v) { return max(max(v.x, v.y), max(v.z, v.w)) == 0xffffffffu; }
__device__ __forceinline__ u32 plo(u32 r1, u32 r0) { return __builtin_amdgcn_perm(r1, r0, 0x05040100u); }
__device__ __forceinline__ u32 phi(u32 r1, u32 r0) { return __builtin_amdgcn_perm(r1, r0, 0x07060302u); }
__device__ __forceinline__ float sum8(const LAS float* p) {
  return ((p[0] + p[64]) + (p[128] + p[192])) + ((p[256] + p[320]) + (p[384] + p[448]));
}
__device__ __forceinline__ void pin(u32& v) { asm volatile("" : "+v"(v)); }
__device__ __forceinline__ void pin4(f32x4& a, f32x4& b, f32x4& c, f32x4& d) { asm volatile("" : "+v"(a), "+v"(b), "+v"(c), "+v"(d)); }
__device__ __forceinline__ void pin(u32x4& v) { asm volatile("" : "+v"(v)); }

__device__ __forceinline__ float red4_parity(float v0, float v1, float v2, float v3) {
  float t = sum16(sum32(v0, v1), sum32(v2, v3));
  t += dppf<XOR2>(t); t += dppf<ROR4>(t); t += dppf<ROR8>(t);
  return t;
}
__device__ __forceinline__ float sum_parity(float t) { t += dppf<XOR2>(t); t += dppf<ROR4>(t); t += dppf<ROR8>(t); t = sum16(t, t); return sum32(t, t); }
__device__ __forceinline__ float max_parity(float t) { t = fmaxf(t, dppf<XOR2>(t)); t = fmaxf(t, dppf<ROR4>(t)); t = fmaxf(t, dppf<ROR8>(t)); return max32(max16(t)); }
__device__ __forceinline__ void ln_stats(u32x4 v, float& mean, float& rden) {
  const float piv = lo16(rfl(v.x));
  f32x2 d[4];
#pragma unroll
  for (int i = 0; i < 4; ++i) d[i] = f32x2{lo16(v[i]), hi16(v[i])} - piv;
  const f32x2 s2 = (d[0] + d[1]) + (d[2] + d[3]), q2 = __builtin_elementwise_fma(d[0], d[0], d[1] * d[1]) + __builtin_elementwise_fma(d[2], d[2], d[3] * d[3]);
  float t = sum32(s2.x + s2.y, q2.x + q2.y);
  t = sum16(t, t);
  t += dppf<XOR1>(t); t += dppf<XOR2>(t); t += dppf<ROR4>(t); t += dppf<ROR8>(t);
  auto r = __builtin_amdgcn_permlane32_swap(asu(t), asu(t), false, false);
  const float ds = asf(r[0]) * (1.f / D), dq = asf(r[1]) * (1.f / D);
  mean = rbf(piv + ds);
  const float var = rbf(fmaxf(fmaf(-ds, ds, dq), 0.f));
  rden = rcp(rbf(__builtin_amdgcn_sqrtf(rbf(var + 1e-5f))));
}
template <int NR> __device__ __forceinline__ void pair_rows(const u32x4 (&w)[NR], u32x4 (&p)[NR]) {
#pragma unroll
  for (int i = 0; i < NR; i += 2) {
    p[i] = u32x4{plo(w[i + 1].x, w[i].x), phi(w[i + 1].x, w[i].x), plo(w[i + 1].y, w[i].y), phi(w[i + 1].y, w[i].y)};
    p[i + 1] = u32x4{plo(w[i + 1].z, w[i].z), phi(w[i + 1].z, w[i].z), plo(w[i + 1].w, w[i].w), phi(w[i + 1].w, w[i].w)};
    pin(p[i]); pin(p[i + 1]);
  }
}
template <int NR> __device__ __forceinline__ void dots(const u32x4 (&p)[NR], const u32 (&x)[NR / 2], float& w0, float& w1) {
  float v[8];
#pragma unroll
  for (int j = 0; j < 8; ++j) { v[j] = 0.f;
#pragma unroll
    for (int i = 0; i < NR / 2; ++i) v[j] = dot2(p[2 * i + (j >> 2)][j & 3], x[i], v[j]); }
  w0 = sum16(sum32(v[0], v[1]), sum32(v[2], v[3])); w1 = sum16(sum32(v[4], v[5]), sum32(v[6], v[7]));
  w0 += dppf<ROR8>(w0); w1 += dppf<ROR8>(w1);
}
__device__ __forceinline__ float gemv_wide(const u32x4 (&p)[8], const u32 (&x)[4], int lane) { float a, b; dots<8>(p, x, a, b); return (lane & 8) ? b : a; }
__device__ __forceinline__ float gemv_narrow(const u32x4 (&p)[4], const u32 (&x)[2], int lane) {
  float a, b; dots<4>(p, x, a, b); a += dppf<ROR4>(a); b += dppf<ROR4>(b); return (lane & 8) ? b : a;
}
__device__ __forceinline__ u32x4 spread8(u32 v, int lane) {
  const u32 pk = __builtin_amdgcn_perm(dppu<XOR1>(v), v, (lane & 1) ? 0x01000504u : 0x05040100u);
  const u32 a = dppu<QB0>(pk), b = dppu<QB2>(pk);
  return u32x4{a, b, dppu<HMIR>(a), dppu<HMIR>(b)};
}
__device__ __forceinline__ void frag2(u32x4 a, u32x4 b, u32x4& c0, u32x4& c1) {
  c0 = u32x4{plo(a.y, a.x), plo(a.w, a.z), plo(b.y, b.x), plo(b.w, b.z)};
  c1 = u32x4{phi(a.y, a.x), phi(a.w, a.z), phi(b.y, b.x), phi(b.w, b.z)};
  pin(c0); pin(c1);
}
__device__ __forceinline__ void frag4(u32x4 a, u32x4 b, u32x4 c, u32x4 d, u32x4 (&f)[4]) {
  frag2(u32x4{a.x, a.z, b.x, b.z}, u32x4{c.x, c.z, d.x, d.z}, f[0], f[1]);
  frag2(u32x4{a.y, a.w, b.y, b.w}, u32x4{c.y, c.w, d.y, d.w}, f[2], f[3]);
}

__global__ void __launch_bounds__(NTHR, 3)
decode_block(const u16* __restrict__ xin, const u16* __restrict__ kc, const u16* __restrict__ vc, const u16* __restrict__ wq,
             const u16* __restrict__ wo, const u16* __restrict__ wg, const u16* __restrict__ wu, const u16* __restrict__ wd,
             const u16* __restrict__ cin, const u16* __restrict__ wmod, u16* __restrict__ out, unsigned char* ws) {
  u32 junk = 0;
  if (threadIdx.x < (CODE + 63) / 64) {
    u64 pc;
    asm volatile("s_getpc_b64 %1\n\tglobal_load_dword %0, %2, %1" : "+v"(junk), "=&s"(pc) : "v"((blockIdx.x >> 3) * CODE + threadIdx.x * 64));
  }
  __shared__ __attribute__((aligned(16))) unsigned char lds_raw[L_END];
  LAS unsigned char* L = (LAS unsigned char*)lds_raw;
  volatile LAS u32* ctl = (volatile LAS u32*)(L + L_CTL);
  const int lane = threadIdx.x & 63, wave = (int)rfl(threadIdx.x >> 6);
  const int b = blockIdx.x, x = b & 7, s = b >> 3, role = s >> 3, ri = s & 7;
  const int rid = 8 * x + ri;
  const int fid = 14 * (x >> 1) + 8 * (x & 1) + ri;
  const int mtask = 4 * x + (ri & 3);
  const bool isF = role == 3 && ri < 8 - 2 * (x & 1);
  const u64 kv_off = (u64)(x * 32 + s) * CHUNK;
#define wsr __builtin_amdgcn_make_buffer_rsrc(ws, 0, WS_END, 0x00020000)
#define WS_LD4(off) __builtin_amdgcn_raw_buffer_load_b128(wsr, (off), 0, 16)
#define WS_LD1(off) __builtin_amdgcn_raw_buffer_load_b32(wsr, (off), 0, 16)
#define WS_ST4(v, off) __builtin_amdgcn_raw_buffer_store_b128((v), wsr, (off), 0, 16)
#define WS_ST2(v, off) __builtin_amdgcn_raw_buffer_store_b64((v), wsr, (off), 0, 16)
#define WS_ST1(v, off) __builtin_amdgcn_raw_buffer_store_b32((v), wsr, (off), 0, 16)
#define WS_G(off) ((GAS u64*)(ws + (off)))
  auto wait_lds = [&](int word, u32 need) {
    SPIN(n, SPIN_LDS) { if (rfl(ctl[word]) >= need) break; __builtin_amdgcn_s_sleep(1); }
    CBAR();
  };

  if (wave == 0) {
    const u32 t0 = now32();
    ctl[C_BASE] = t0;
    const u64 ka = (u64)kc + kv_off, va = (u64)vc + kv_off;
    const u32 lbase = (u32)(uintptr_t)L + L_RING;
    const int r = lane >> 4, pos = lane & 15;
    LGKM0();
    __builtin_amdgcn_s_barrier();
    __builtin_amdgcn_s_setprio(3);
    asm volatile("s_waitcnt vmcnt(0)" : "+v"(junk));
    u32 tnext = (u32)T_START << 16, slot = 0, pub = 0, now = 0, room = NS, known = 0;
    auto freed = [&]() {
      u32 d = ctl[C_DONE + (lane & 7)];
      d = min(d, dppu<XOR1>(d)); d = min(d, dppu<XOR2>(d)); d = min(d, dppu<HMIR>(d));
      return rfl(d);
    };
#pragma unroll 1
    for (int ph = 0; ph < 2; ++ph) {
      const u64 a = ph ? va : ka;
      const u32x4 rs = {(u32)a, (u32)(a >> 32) & 0xffffu, (u32)CHUNK, 0x00020000u};
      const int sw = ph ? 0 : 15;
      const u32 v0 = r * 256 + ((pos ^ (r & sw)) << 4), v1 = r * 256 + ((pos ^ ((4 + r) & sw)) << 4),
                v2 = r * 256 + ((pos ^ ((8 + r) & sw)) << 4), v3 = r * 256 + ((pos ^ ((12 + r) & sw)) << 4);
      const int tend = ph ? NTILE - ND : NTILE;
      int skip = ph ? NTILE - 8 * (ND - 1) - 1 : -1;
      u32 goff = 0;
#pragma unroll 1
      for (int t = 0; t < tend; t += G) {
        const u32 tile = ph * NTILE + t;
        if (!known && (known = rfl(ctl[C_SKEW]))) tnext += (u32)max(stream_shift((int)known - SKEW0), -(int)(tnext >> 16)) << 16;
        const bool late = now >= (tnext >> 16);
        while (now < (tnext >> 16)) now = now32() - t0;
        tnext += tile < SLOW_N ? TSTEP + TSLOW : TSTEP;
        if (tile + G > room) {
          room = freed() + NS;
          if (tile + G > room) {
            VM0(); pub = tile; ctl[C_LANDED] = pub;
            SPIN(n, SPIN_LDS) { __builtin_amdgcn_s_sleep(1); room = freed() + NS; if (tile + G <= room) break; }
          }
        }
        u32 lag;
        if (late) { asm volatile("s_waitcnt vmcnt(%0)" ::"n"(WMAX - 4 * G) : "memory"); lag = WMAX / 4 - G; }
        else { asm volatile("s_waitcnt vmcnt(%0)" ::"n"(WPUB) : "memory"); lag = WPUB / 4; }
        if (tile > pub + lag) { pub = tile - lag; ctl[C_LANDED] = pub; }
#pragma unroll
        for (int i = 0; i < G; ++i) {
          asm volatile("s_mov_b32 m0, %0\n\ts_nop 0\n\t"
                       "buffer_load_dwordx4 %1, %5, %6 offen nt lds\n\t"
                       "buffer_load_dwordx4 %2, %5, %6 offen offset:1024 nt lds\n\t"
                       "buffer_load_dwordx4 %3, %5, %6 offen offset:2048 nt lds\n\t"
                       "buffer_load_dwordx4 %4, %5, %6 offen offset:3072 nt lds"
                       ::"s"(lbase + (slot + i) * 4096), "v"(v0), "v"(v1), "v"(v2), "v"(v3), "s"(rs),
                         "s"(goff) : "memory");
          const bool jump = t + i + 1 == skip;
          goff = rfl(goff + (jump ? 8192 : 4096)); skip += jump ? 7 : 0;
        }
        slot += G; if (slot >= NS) slot = 0;
      }
    }
#pragma unroll
    for (int k = WMAX / 4 - 1; k >= 0; --k) {
      asm volatile("s_waitcnt vmcnt(%0)" ::"n"(4 * k) : "memory");
      if (2 * NTILE - ND - k > pub) ctl[C_LANDED] = 2 * NTILE - ND - k;
    }
    return;
  }

  if (wave == 1) {
    if (lane < C_N) ctl[lane] = lane < NCW ? lane : 0;
    LGKM0();
    __builtin_amdgcn_s_barrier();
    SBAR();
    const u32 t0 = rfl(ctl[C_BASE]);
    const u64 ep = __hip_atomic_load(WS_G(WS_EPOCH), RLX_AGENT);
    const int j8 = 4 * (lane & 1) + (lane >> 5) + 2 * ((lane >> 4) & 1);
    const u32 xraw = *(const GAS u16*)(xin + 8 * rid + j8);
    const u32x4 ones = {~0u, ~0u, ~0u, ~0u};
    if (b == 0 && lane == 0) WS_ST1(0u, WS_LATE);
    if (lane < 2) WS_ST4(ones, WS_ML + x * 1024 + s * 32 + lane * 16);
    if (role == 0) {
      const u32x4 tail = lane < 48 ? ones : u32x4{0u, 0u, 0u, 0u};
      if (ri >= 4 && lane < 8) WS_ST4(ones, WS_MOD + mtask * 128 + lane * 16);
      WS_ST4(ones, WS_M2 + rid * 2048 + lane * 16); WS_ST4(ones, WS_M2 + rid * 2048 + 1024 + lane * 16);
      WS_ST4(ones, WS_M4 + rid * 2048 + lane * 16); WS_ST4(tail, WS_M4 + rid * 2048 + 1024 + lane * 16);
    } else if (role == 2) {
#pragma unroll
      for (int i = 0; i < 8; ++i) WS_ST4(ones, WS_O + rid * 8192 + i * 1024 + lane * 16);
    } else if (isF) {
      WS_ST4(ones, WS_X1 + fid * 1024 + lane * 16);
    }
    SBAR();
    const u64 tag = ep + 1;
    if (s == 0 && lane == 0) __hip_atomic_store(WS_G(WS_T0 + x * 128), (tag << 32) | t0, RLX_AGENT);
    VM0();
    if (lane == 0) __hip_atomic_store(WS_G(WS_READY + b * 8), tag, RLX_AGENT);
    int skew;
    {
      int d;
      for (u32 t = T_T0;; t += 60) {
        until(t0 + t);
        const u64 e = __hip_atomic_load(WS_G(WS_T0 + (lane & 7) * 128), RLX_AGENT);
        const bool ok = (u32)(e >> 32) == (u32)tag;
        d = ok ? (int)((u32)e - t0) : -1000;
        if (__all(ok) || t > T_T0 + 200) break;
      }
      int e = d;
      d = max(d, (int)dppu<XOR1>(d)); d = max(d, (int)dppu<XOR2>(d)); d = max(d, (int)dppu<HMIR>(d));
      e = min(e, (int)dppu<XOR1>(e)); e = min(e, (int)dppu<XOR2>(e)); e = min(e, (int)dppu<HMIR>(e));
      skew = (int)rfl(d - min((d - e) >> 1, T_CATCH));
      skew = skew < -300 || skew > 300 ? 0 : skew;
      ctl[C_SKEW] = skew + SKEW0;
    }
    const u32 tail0 = t0 + skew + T_LAST;
    if (role == 0 && ri >= 4) {
      wait_lds(C_MODRDY, 1);
      const f32x2 mv = *(const LAS f32x2*)(L + L_MODV + (lane & 31) * 8);
      if (lane < 32) WS_ST1(canon(pk2(mv.x, mv.y)), WS_MOD + mtask * 128 + lane * 4);
    }
    {
      const u32 thi = (u32)(tag >> 16), tlo = (u32)tag & 0xffffu;
      u64 g[8];
      bool rdy;
      while (now32() - t0 < (u32)(T_Q + skew)) __builtin_amdgcn_s_sleep(2);
      SPIN(n, SPIN_MEM) {
        bool ok = true;
#pragma unroll
        for (int i = 0; i < 8; ++i) g[i] = __hip_atomic_load(WS_G(WS_Q + x * 4096 + i * 512 + lane * 8), RLX_AGENT);
        rdy = true;
#pragma unroll
        for (int i = 0; i < 4; ++i) rdy &= __hip_atomic_load(WS_G(WS_READY + (i * 64 + lane) * 8), RLX_AGENT) == tag;
#pragma unroll
        for (int i = 0; i < 8; ++i) ok &= (u32)(g[i] >> 32) == thi && ((u32)g[i] >> 16) == tlo;
        if (__all(ok && rdy)) break;
        __builtin_amdgcn_s_sleep(RETRY);
      }
#pragma unroll
      for (int i = 0; i < 8; ++i) *(LAS u16*)(L + L_QB + i * 128 + lane * 2) = (u16)g[i];
      CBAR();
      ctl[C_QRDY] = 1;
    }
    float ga, gb;
    {
      SPIN(n, SPIN_LDS) { if (rfl(ctl[C_MLCNT]) >= NCW) break; __builtin_amdgcn_s_sleep(8); }
      CBAR();
      const f32x2 ml = *(const LAS f32x2*)(L + L_ML + (lane & 31) * 8);
      float m = fmaxf(ml.x, dppf<ROR4>(ml.x)); m = fmaxf(m, dppf<ROR8>(m)); m = max16(m);
      float l = ml.y * ex2((ml.x - m) * LOG2E);
      l += dppf<ROR4>(l); l += dppf<ROR8>(l); l = sum16(l, l);
      const u32x2 v = {canon(asu(m)), canon(asu(l))};
      if (lane < 4) WS_ST2(v, WS_ML + x * 1024 + s * 32 + lane * 8);
      const u32 t1 = now32();
      while (now32() - t1 < T_ML) __builtin_amdgcn_s_sleep(16);
      u32x4 g;
      u32 g0 = 0, g1 = 0;
      SPIN(n, SPIN_MEM) {
        CBAR();
        g = WS_LD4(WS_ML + x * 1024 + lane * 16);
        if (role == 0) { g0 = WS_LD1(WS_MOD + rid * 16 + (j8 >> 1) * 4); g1 = WS_LD1(WS_MOD + 3072 + rid * 16 + (j8 >> 1) * 4); }
        if (!__any(bad4(g) || max(g0, g1) == 0xffffffffu)) break;
        __builtin_amdgcn_s_sleep(RETRY);
      }
      ga = (j8 & 1) ? hi16(g0) : lo16(g0); gb = (j8 & 1) ? hi16(g1) : lo16(g1);
      const float ma = max_parity(asf(g.x)), mb = max_parity(asf(g.z));
      const float la = sum_parity(asf(g.y) * ex2((asf(g.x) - ma) * LOG2E)), lb = sum_parity(asf(g.w) * ex2((asf(g.z) - mb) * LOG2E));
      if (lane < 2) *(LAS f32x4*)(L + L_FF + lane * 16) = f32x4{ma, 1.f / la, mb, 1.f / lb};
      CBAR();
      ctl[C_FRDY] = 1;
    }
    if (role != 0) return;
    LGKM0();
    __builtin_amdgcn_s_barrier();
    auto gather64 = [&](u32 off, u32 t) {
      u32x4 g0, g1;
      SPIN(n, SPIN_MEM) {
        until(t); t += T_AGAIN;
        CBAR();
        g0 = WS_LD4(off); g1 = WS_LD4(off + 1024);
        if (!__any(bad4(g0) || bad4(g1))) break;
      }
      return red4_parity(asf(g0.x) + asf(g1.x), asf(g0.y) + asf(g1.y), asf(g0.z) + asf(g1.z), asf(g0.w) + asf(g1.w));
    };
    u32 o2 = WS_M2 + rid * 2048 + lane * 16, o4 = WS_M4 + rid * 2048 + lane * 16, ox = WS_X1 + lane * 1024 + rid * 16, oo = (8 * rid + j8) * 2;
    pin(o2); pin(o4); pin(ox); pin(oo);
    float xres = lo16(xraw);
    asm volatile("" : "+v"(ga), "+v"(gb), "+v"(xres));
    until(tail0 + T_B - 40);
    CBAR();
    const u32 late = min(rfl(WS_LD1(WS_LATE)), 400u);
    const float att = gather64(o2, tail0 + late + T_B);
    const float x1 = rbf(xres + rbf(ga * rbf(att)));
    {
      auto t = __builtin_amdgcn_permlane32_swap(asu(x1), asu(x1), false, false);
      const u32 pk = canon(phi(t[1], t[0]));
      auto u = __builtin_amdgcn_permlane16_swap(pk, pk, false, false);
      const u32 n0 = dppu<XOR1>(u[0]), n1 = dppu<XOR1>(u[1]);
      const u32x4 v = (lane & 1) ? u32x4{n0, n1, u[0], u[1]} : u32x4{u[0], u[1], n0, n1};
      if (lane < NF) WS_ST4(v, ox);
    }
    if (b == 0 && lane == 0) __hip_atomic_store(WS_G(WS_EPOCH), tag, RLX_AGENT);
    const float y = gather64(o4, tail0 + late + T_D);
    if ((lane & 14) == 0) *(GAS u16*)((GAS unsigned char*)out + oo) = (u16)bfbits(x1 + rbf(gb * rbf(y)));
    return;
  }

  const int w = wave - 2, g = lane >> 4, i16 = lane & 15, a8 = lane >> 3, ch = lane & 7, b2 = (lane >> 2) & 1;
  const int jcol = (lane >> 5) + 2 * ((lane >> 4) & 1) + 4 * ((lane >> 3) & 1);
  LAS float* red = (LAS float*)(L + L_RED);
  auto last_arriver = [&]() {
    CBAR();
    u32 old = 0;
    if (lane == 0) old = __hip_atomic_fetch_add((LAS u32*)(L + L_CTL) + C_CNT1, 1u, RLX_WG);
    const bool last = rfl(old) == NCW - 1;
    CBAR();
    return last;
  };
  u32x4 wp[8];
  u32x4 wu0[2];
  u64 tag = 0;
  float lnx = 0.f;
  const int r4 = 64 * w + 4 * (lane >> 2), qtask = 16 * x + (role == 3 ? 8 : 0) + ri;
  if (role == 2) {
    SBAR();
    __builtin_amdgcn_s_barrier();
    SBAR();
#pragma unroll
    for (int i = 0; i < 8; ++i) {
      const u16* p = wo + (size_t)(64 * rid + 32 * (i >> 2) + 8 * g + 2 * (i & 3)) * D + 64 * w + 4 * i16;
      const u32x2 r0 = ldnt<u32x2>(p), r1 = ldnt<u32x2>(p + D);
      wp[i] = u32x4{r0.x, r0.y, r1.x, r1.y};
    }
  } else if (role == 0 && ri >= 4) {
    u32x4 wn[8];
    SBAR();
    __builtin_amdgcn_s_barrier();
    SBAR();
#pragma unroll
    for (int i = 0; i < 8; ++i) wn[i] = ldnt<u32x4>(wmod + (size_t)(64 * w + 8 * a8 + i) * (6 * D) + 2 * D + 64 * mtask + 8 * ch);
    SBAR();
    const u32x4 craw = *(const GAS u32x4*)(cin + 64 * w + 8 * a8);
    SBAR();
    u32 xs[4];
#pragma unroll
    for (int i = 0; i < 4; ++i) xs[i] = pk2(silu(lo16(craw[i])), silu(hi16(craw[i])));
    pair_rows<8>(wn, wp);
    red[w * 64 + 8 * ch + jcol] = gemv_wide(wp, xs, lane);
    if (last_arriver()) {
      float t = sum8(red + lane);
      t = rbf(t);
      if ((mtask >> 3) == 2) t = rbf(1.f + t);
      ((LAS float*)(L + L_MODV))[lane] = t;
      CBAR();
      ctl[C_MODRDY] = 1;
    }
  } else if (role != 0) {
    u32x4 wn[4];
    const u16* p0 = wq + (size_t)r4 * NQ + 32 * qtask + 8 * (lane & 3);
    wn[0] = ldnt<u32x4>(p0); wn[1] = ldnt<u32x4>(p0 + NQ);
    SBAR();
    __builtin_amdgcn_s_barrier();
    SBAR();
    wn[2] = ldnt<u32x4>(p0 + 2 * NQ); wn[3] = ldnt<u32x4>(p0 + 3 * NQ);
    SBAR();
    const u32x4 xv = *(const GAS u32x4*)(xin + lane * 8);
    const u32 xraw = *(const GAS u16*)(xin + 64 * w + lane);
    const u64 ep = __hip_atomic_load(WS_G(WS_EPOCH), RLX_AGENT);
    SBAR();
    if (isF) {
      const u16* p = wg + (size_t)(64 * w + 8 * g) * FF + 32 * fid + 2 * i16;
#pragma unroll
      for (int i = 0; i < 4; ++i) {
        const u16* q = p + (32 * (i >> 1) + 4 * (i & 1)) * FF;
        wp[4 + i] = u32x4{ldnt<u32>(q), ldnt<u32>(q + FF), ldnt<u32>(q + 2 * FF), ldnt<u32>(q + 3 * FF)};
      }
#pragma unroll
      for (int i = 0; i < 2; ++i) {
        const u16* q = wu + (p - wg) + 4 * i * FF;
        wu0[i] = u32x4{ldnt<u32>(q), ldnt<u32>(q + FF), ldnt<u32>(q + 2 * FF), ldnt<u32>(q + 3 * FF)};
      }
    }
    SBAR();
    float mean, rden;
    ln_stats(xv, mean, rden);
    lnx = rbf(rbf(lo16(xraw) - mean) * rden);
    asm volatile("" : "+v"(lnx));
    tag = (((u64)rfl((u32)(ep >> 32)) << 32) | rfl((u32)ep)) + 1;
    pair_rows<4>(wn, (u32x4 (&)[4])wp[0]);
  } else {
    u32x4 wn[4], wp2[4];
    const u16* p = wmod + (size_t)r4 * (6 * D) + 32 * mtask + 8 * (lane & 3);
    wn[0] = ldnt<u32x4>(p); wn[1] = ldnt<u32x4>(p + 6 * D);
    SBAR();
    __builtin_amdgcn_s_barrier();
    SBAR();
    wn[2] = ldnt<u32x4>(p + 12 * D); wn[3] = ldnt<u32x4>(p + 18 * D);
    SBAR();
    const u32x2 craw = *(const GAS u32x2*)(cin + r4);
    const u64 ep = __hip_atomic_load(WS_G(WS_EPOCH), RLX_AGENT);
    SBAR();
    const u32 xs[2] = {pk2(silu(lo16(craw.x)), silu(hi16(craw.x))), pk2(silu(lo16(craw.y)), silu(hi16(craw.y)))};
    pair_rows<4>(wn, wp2);
    red[w * 64 + 8 * (lane & 3) + jcol] = gemv_narrow(wp2, xs, lane);
    if (last_arriver()) {
      float t = sum8(red + (lane & 31));
      t = rbf(t);
      if (mtask >= 16) t = rbf(1.f + t);
      if (lane < 32) __hip_atomic_store(WS_G(WS_H + (32 * mtask + lane) * 8), ((ep + 1) << 16) | (asu(t) >> 16), RLX_AGENT);
    }
  }
  const u32 tw0 = rfl(ctl[C_BASE]);
  auto q_task = [&]() {
    u64 sg, cg;
    wait_lds(C_SKEW, 1);
    const u32 th = T_H + rfl(ctl[C_SKEW]) - SKEW0;
    while (now32() - tw0 < th) __builtin_amdgcn_s_sleep(1);
    SPIN(n, SPIN_MEM) {
      sg = __hip_atomic_load(WS_G(WS_H + (64 * w + lane) * 8), RLX_AGENT);
      cg = __hip_atomic_load(WS_G(WS_H + 4096 + (64 * w + lane) * 8), RLX_AGENT);
      if (__all((sg >> 16) == (tag & 0xffffffffffffull) && (cg >> 16) == (tag & 0xffffffffffffull))) break;
      __builtin_amdgcn_s_sleep(RETRY / 2);
    }
    const u32 h = pk2(rbf(lnx * lo16((u32)cg)) + lo16((u32)sg), 0.f);
    const u32 pk = __builtin_amdgcn_perm(dppu<XOR1>(h), h, (lane & 1) ? 0x01000504u : 0x05040100u);
    const u32 xs[2] = {dppu<QB0>(pk), dppu<QB2>(pk)};
    red[w * 64 + 8 * (lane & 3) + jcol] = gemv_narrow((const u32x4 (&)[4])wp[0], xs, lane);
    if (last_arriver()) {
      float t = sum8(red + (lane & 31));
      if (lane < 32) __hip_atomic_store(WS_G(WS_Q + (32 * qtask + lane) * 8), (tag << 16) | bfbits(t), RLX_AGENT);
    }
  };

  const int head = i16 >> 2;
  LAS unsigned char* sc = L + L_SC + w * 2048;
  u32 slot = w;
  auto fetch = [&](u32 pos, u32 o0, u32 o1, u32 o2, u32 o3, u32x4 (&f)[4]) {
    wait_lds(C_LANDED, pos + 1);
    const LAS unsigned char* p = L + L_RING + slot * 4096;
    f[0] = *(const LAS u32x4*)(p + o0); f[1] = *(const LAS u32x4*)(p + o1); f[2] = *(const LAS u32x4*)(p + o2); f[3] = *(const LAS u32x4*)(p + o3);
    SBAR();
    asm volatile("s_waitcnt lgkmcnt(0)" : "+v"(f[0]), "+v"(f[1]), "+v"(f[2]), "+v"(f[3]) :: "memory");
    ctl[C_DONE + w] = pos + NCW;
    slot += NCW; if (slot >= NS) slot -= NS;
  };
  float m_w;
  {
    const u32 ko = i16 * 256 + ((g ^ i16) << 4);
    static_assert(QAT >= 1 && QAT <= PRE, "the Q task runs between two parked tiles");
    u32x4 pre[PRE][4];
#pragma unroll
    for (int i = 0; i < PRE; ++i) {
      fetch(i * NCW + w, ko, ko ^ 64, ko ^ 128, ko ^ 192, pre[i]);
      if (i == QAT - 1 && (role & 1)) { q_task(); wp[0] = wu0[0]; wp[1] = wu0[1]; pin(wp[0]); pin(wp[1]); }
    }
    wait_lds(C_QRDY, 1);
    u32x4 qf[4];
#pragma unroll
    for (int ks = 0; ks < 4; ++ks) qf[ks] = *(const LAS u32x4*)(L + L_QB + head * 256 + ks * 64 + g * 16);
    float mx = -INFINITY;
    auto score = [&](int i, const u32x4 (&kf)[4]) {
      f32x4 d = {0.f, 0.f, 0.f, 0.f};
#pragma unroll
      for (int ks = 0; ks < 4; ++ks) d = mfma32(kf[ks], qf[ks], d);
      mx = fmaxf(fmaxf(mx, fmaxf(d.x, d.y)), fmaxf(d.z, d.w));
      const u32 r0 = pk2(d.x, d.y), r1 = pk2(d.z, d.w);
      const u32x2 z = {pk2(lo16(r0) * SCALE, hi16(r0) * SCALE), pk2(lo16(r1) * SCALE, hi16(r1) * SCALE)};
      if ((lane & 3) == 0) *(LAS u32x2*)(sc + i * 128 + g * 32 + head * 8) = z;
    };
#pragma unroll
    for (int i = 0; i < PRE; ++i) score(i, pre[i]);
#pragma unroll 1
    for (int i = PRE; i < NIT; ++i) {
      u32x4 kf[4];
      fetch(i * NCW + w, ko, ko ^ 64, ko ^ 128, ko ^ 192, kf);
      score(i, kf);
    }
    m_w = rbf(rbf(max32(max16(mx))) * SCALE);
  }
  __builtin_amdgcn_s_waitcnt(0x0F70);
  {
    const u32 mu = asu(m_w);
    const float m0 = asf(__builtin_amdgcn_readlane(mu, 0)), m1 = asf(__builtin_amdgcn_readlane(mu, 4)),
                m2 = asf(__builtin_amdgcn_readlane(mu, 8)), m3 = asf(__builtin_amdgcn_readlane(mu, 12));
    const float ma = (lane & 1) ? m2 : m0, mb = (lane & 1) ? m3 : m1;
    float la = 0.f, lb = 0.f;
    CBAR();
#pragma unroll
    for (int k = 0; k < 2; ++k) {
      u32x4 v = *(const LAS u32x4*)(sc + k * 1024 + lane * 16);
#pragma unroll
      for (int d = 0; d < 4; ++d) {
        const float mm = d < 2 ? ma : mb;
        const float p0 = ex2((lo16(v[d]) - mm) * LOG2E), p1 = ex2((hi16(v[d]) - mm) * LOG2E);
        if (d < 2) la += p0 + p1; else lb += p0 + p1;
        v[d] = pk2(p0, p1);
      }
      *(LAS u32x4*)(sc + k * 1024 + lane * 16) = v;
    }
    la = sum_parity(la); lb = sum_parity(lb);
    if (lane < 2) *(LAS f32x4*)(L + L_ML + w * 32 + lane * 16) = f32x4{ma, la, mb, lb};
    CBAR();
    if (lane == 0) __hip_atomic_fetch_add((LAS u32*)(L + L_CTL) + C_MLCNT, 1u, RLX_WG);
    m_w = g == 0 ? m0 : g == 1 ? m1 : g == 2 ? m2 : m3;
  }
  f32x4 acc0 = {0.f, 0.f, 0.f, 0.f}, acc1 = {0.f, 0.f, 0.f, 0.f};
  const int js = lane & 3;
  const u32 sx0 = js == 0 ? 0x0c0c0100u : js == 1 ? 0x01000c0cu : 0x0c0c0c0cu, sx1 = js == 0 ? 0x0c0c0302u : js == 1 ? 0x03020c0cu : 0x0c0c0c0cu;
  const u32 sy0 = js == 2 ? 0x0c0c0100u : js == 3 ? 0x01000c0cu : 0x0c0c0c0cu, sy1 = js == 2 ? 0x0c0c0302u : js == 3 ? 0x03020c0cu : 0x0c0c0c0cu;
  const u32 vo = g * 1024 + i16 * 16;
  auto p_frags = [&](int i, u32x2 (&pa)[4]) {
    const u32x2 p = *(const LAS u32x2*)(sc + i * 128 + g * 32 + head * 8);
#pragma unroll
    for (int t = 0; t < 4; ++t) pa[t] = u32x2{__builtin_amdgcn_perm(0u, p[t >> 1], (t & 1) ? sx1 : sx0), __builtin_amdgcn_perm(0u, p[t >> 1], (t & 1) ? sy1 : sy0)};
  };
  auto pv = [&](const u32x2 (&pa)[4], const u32x4 (&vf)[4]) {
#pragma unroll
    for (int t = 0; t < 4; ++t) { acc0 = mfma16(pa[t], u32x2{vf[t].x, vf[t].y}, acc0); acc1 = mfma16(pa[t], u32x2{vf[t].z, vf[t].w}, acc1); }
  };
  auto vtile = [&](int i, u32 pos) {
    u32x2 pa[4]; u32x4 vf[4];
    p_frags(i, pa);
    fetch(pos, vo, vo + 256, vo + 512, vo + 768, vf);
    pv(pa, vf);
  };
#pragma unroll 1
  for (int i = 0; i < NIT - ND; ++i) vtile(i, NTILE + i * NCW + w);
  wait_lds(C_FRDY, 1);
  const f32x2 ff = *(const LAS f32x2*)(L + L_FF + g * 8);
  float fac = ex2((m_w - ff.x) * LOG2E) * ff.y;
  asm volatile("" : "+v"(fac));
  if (role == 2) {
    u32x4 f0[4], f1[4];
    frag4(wp[0], wp[1], wp[2], wp[3], f0); frag4(wp[4], wp[5], wp[6], wp[7], f1);
#pragma unroll
    for (int j = 0; j < 4; ++j) { wp[j] = f0[j]; wp[4 + j] = f1[j]; }
  } else if (isF) {
#pragma unroll
    for (int j = 4; j < 8; j += 2) { u32x4 c0, c1; frag2(wp[j], wp[j + 1], c0, c1); wp[j] = c0; wp[j + 1] = c1; }
    { u32x4 c0, c1; frag2(wp[0], wp[1], c0, c1); wp[0] = c0; wp[1] = c1; }
  }
  const int skew = (int)rfl(ctl[C_SKEW]) - SKEW0;
  const u32 tailw = tw0 + skew + T_LAST;
  const int frow = 64 * w + 32 * (a8 & 1) + 8 * g + ch;
  u32 c1v = 0, shv = 0;
  if (isF) {
    SPIN(n, SPIN_MEM) {
      CBAR();
      c1v = WS_LD1(WS_MOD + 2048 + (frow >> 1) * 4); shv = WS_LD1(WS_MOD + 1024 + (frow >> 1) * 4);
      if (!__any(max(c1v, shv) == 0xffffffffu)) break;
      __builtin_amdgcn_s_sleep(RETRY);
    }
  }
  if (w < NCW - 1) {
#pragma unroll 1
    for (int i = NIT - ND; i < NIT; ++i) {
      vtile(i, NTILE + i * NCW + w - (i - (NIT - ND)));
      slot = slot ? slot - 1 : NS - 1;
    }
    *(LAS f32x4*)(sc + g * 512 + i16 * 32) = acc0 * fac;
    *(LAS f32x4*)(sc + g * 512 + i16 * 32 + 16) = acc1 * fac;
    CBAR();
    if (lane == 0) __hip_atomic_fetch_add((LAS u32*)(L + L_CTL) + C_MRG, 1u, RLX_WG);
  } else {
    u32 off = WS_O + (8 * x + 2 * g + (i16 >> 3)) * 8192 + ch * 1024 + s * 32;
    pin(off);
    u32x2 pa[2][4];
    u32x4 va[ND][4];
    const unsigned char* vp = (const unsigned char*)vc + kv_off + vo;

#pragma unroll
    for (int k = 0; k < ND; ++k) {
      if (k % G == 0) until(tw0 + stream_shift(skew) + t_group((2 * NTILE - ND + k) / G));
#pragma unroll
      for (int t = 0; t < 4; ++t) va[k][t] = ldnt<u32x4>(vp + (NTILE - 1 - 8 * (ND - 1 - k)) * 4096 + t * 256);
      SBAR();
    }
#pragma unroll
    for (int k = 0; k < ND - 2; ++k) { p_frags(NIT - ND + k, pa[0]); pv(pa[0], va[k]); }
    p_frags(NIT - 2, pa[0]); p_frags(NIT - 1, pa[1]);
    asm volatile("" : "+v"(pa[0][0]), "+v"(pa[0][1]), "+v"(pa[0][2]), "+v"(pa[0][3]), "+v"(pa[1][0]), "+v"(pa[1][1]), "+v"(pa[1][2]), "+v"(pa[1][3]), "+v"(acc0), "+v"(acc1));
    SBAR();
    SPIN(n, SPIN_LDS) { if (rfl(ctl[C_MRG]) >= NCW - 1) break; }
    CBAR();
    f32x4 o0 = {0.f, 0.f, 0.f, 0.f}, o1 = {0.f, 0.f, 0.f, 0.f};
#pragma unroll
    for (int i = 0; i < NCW - 1; ++i) {
      o0 += *(const LAS f32x4*)(L + L_SC + i * 2048 + g * 512 + i16 * 32); o1 += *(const LAS f32x4*)(L + L_SC + i * 2048 + g * 512 + i16 * 32 + 16);
    }
    asm volatile("" : "+v"(o0), "+v"(o1));
    SBAR();
    pv(pa[0], va[ND - 2]); pv(pa[1], va[ND - 1]);
    o0 += acc0 * fac; o1 += acc1 * fac;
    WS_ST4(canon4(o0.x, o0.y, o0.z, o0.w), off); WS_ST4(canon4(o1.x, o1.y, o1.z, o1.w), off + 16);
    const int d = (int)(now32() - tailw) - T_H1;
    if (d > 0 && lane == 0) __hip_atomic_fetch_max((GAS u32*)(ws + WS_LATE), (u32)d + 25u, RLX_AGENT);
  }

  if (role == 2) {
    u32 oi = WS_O + rid * 8192 + w * 1024 + lane * 16, os = WS_M2 + (8 * w + (i16 >> 1)) * 2048 + rid * 32 + (lane & 1) * 16,
        ol = L_OPK + w * 16 + (4 * (lane & 1) + (lane >> 5) + 2 * ((lane >> 4) & 1)) * 2, or_ = L_OPK + g * 16;
    pin(oi); pin(os); pin(ol); pin(or_);
    __builtin_amdgcn_s_barrier();
    const u32 ta = tailw + T_A;
    u32x4 v;
    SPIN(n, SPIN_MEM) {
      until(ta + n * T_AGAIN);
      CBAR();
      v = WS_LD4(oi);
      if (!__any(bad4(v))) break;
    }
    const float o = red4_parity(asf(v.x), asf(v.y), asf(v.z), asf(v.w));
    if ((lane & 14) == 0) *(LAS u16*)(L + ol) = (u16)bfbits(o);
    LGKM0();
    __builtin_amdgcn_s_barrier();
    CBAR();
    const u32x4 x0 = *(const LAS u32x4*)(L + or_), x1 = *(const LAS u32x4*)(L + or_ + 64);
    f32x4 d[4];
#pragma unroll
    for (int j = 0; j < 4; ++j) d[j] = mfma32(x0, wp[j], f32x4{0.f, 0.f, 0.f, 0.f});
#pragma unroll
    for (int j = 0; j < 4; ++j) d[j] = mfma32(x1, wp[4 + j], d[j]);
    pin4(d[0], d[1], d[2], d[3]);
    if (lane < 16) WS_ST4(canon4(d[0].x, d[1].x, d[2].x, d[3].x), os);
  } else if (isF) {
#define DMA1 "s_mov_b32 m0, %3\n\ts_nop 0\n\tbuffer_load_dwordx4 %1, %0, %2 offen nt lds"
    const u32 lb = L_RING + w * 8192, m = rfl((u32)(uintptr_t)L + lb);
    const u64 ua = (u64)wu + 64 * fid, da = (u64)wd + (u64)fid * 32 * D * 2;
    const u32x4 ru = {(u32)ua, (u32)(ua >> 32) & 0xffffu, D * FF * 2 - 64u * fid, 0x00020000u}, rd = {(u32)da, (u32)(da >> 32) & 0xffffu, 32 * D * 2, 0x00020000u};
    const u32x4 rw = {(u32)(uintptr_t)ws, (u32)((uintptr_t)ws >> 32) & 0xffffu, WS_END, 0x00020000u};
    const u32 vu = (64 * w + 2 * (lane >> 2)) * (FF * 2) + (lane & 3) * 16, vd = a8 * (D * 2) + w * 128 + ch * 16;
    __builtin_amdgcn_s_barrier();
#pragma unroll
    for (int i = 1; i < 4; i += 2) asm volatile(DMA1 ::"s"(ru), "v"(vu), "s"((32 * (i & 1) + (i >> 1)) * FF * 2), "s"(m + i * 1024) : "memory");
#pragma unroll
    for (int i = 0; i < 4; ++i) asm volatile(DMA1 ::"s"(rd), "v"(vd), "s"(i * 8 * D * 2), "s"(m + 4096 + i * 1024) : "memory");
    u32 lraw;
    asm volatile("buffer_load_dword %0, off, %1, 0 offset:%2 sc1" : "=v"(lraw) : "s"(rw), "n"(WS_LATE) : "memory");
    const u32 sh16 = (ch & 1) ? 0u : 16u;
    float c1 = hi16(c1v << sh16), sh = hi16(shv << sh16);
    asm volatile("s_waitcnt vmcnt(5)" : "+v"(c1), "+v"(sh) :: "memory");
#pragma unroll
    for (int ks = 1; ks < 2; ++ks) {
      const LAS u32* e = (const LAS u32*)(L + lb + ks * 1024 + g * 256 + i16 * 4);
      frag2(u32x4{e[0], e[512], e[16], e[528]}, u32x4{e[32], e[544], e[48], e[560]}, wp[2 * ks], wp[2 * ks + 1]);
    }
    u32 oa = WS_X1 + fid * 1024 + lane * 16, ob = WS_X1 + fid * 1024 + (frow >> 3) * 16 + (ch >> 1) * 4;
    u32 os = WS_M4 + (8 * w + (i16 >> 1)) * 2048 + fid * 32 + (lane & 1) * 16, ol = L_RED + w * 256 + lane * 16,
        or_ = L_RED + (16 * g + 4 * (ch >> 1) + (ch & 1) + 2 * (a8 & 1)) * 4;
    pin(oa); pin(ob); pin(os); pin(ol); pin(or_);
    asm volatile("s_waitcnt vmcnt(0)" : "+v"(lraw) :: "memory");
    auto col4 = [&](u32 o) { const LAS u32x2* p = (const LAS u32x2*)(L + o + g * 1024 + i16 * 8); const u32x2 r0 = p[0], r1 = p[16]; return u32x4{r0.x, r0.y, r1.x, r1.y}; };
    u32x4 fd[4];
    frag4(col4(lb + 4096), col4(lb + 4352), col4(lb + 4608), col4(lb + 4864), fd);
    const u32 late = min(rfl(lraw), 400u);
    u32x4 xa; u32 xb;
    SPIN(n, SPIN_MEM) {
      until(tailw + late + T_C + n * T_AGAIN);
      CBAR();
      xa = WS_LD4(oa); xb = WS_LD1(ob);
      if (!__any(bad4(xa) || xb == 0xffffffffu)) break;
    }
    float mean, rden;
    ln_stats(xa, mean, rden);
    const u32x4 x0 = spread8(pk2(rbf(rbf(rbf(hi16(xb << sh16) - mean) * rden) * c1) + sh, 0.f), lane);
    const u32x4 x1 = {dppu<ROR8>(x0.x), dppu<ROR8>(x0.y), dppu<ROR8>(x0.z), dppu<ROR8>(x0.w)};
    const f32x4 z = {0.f, 0.f, 0.f, 0.f};
    f32x4 d0 = mfma32(x0, wp[4], z), d1 = mfma32(x0, wp[5], z), d2 = mfma32(x0, wp[0], z), d3 = mfma32(x0, wp[1], z);
    d0 = mfma32(x1, wp[6], d0); d1 = mfma32(x1, wp[7], d1); d2 = mfma32(x1, wp[2], d2); d3 = mfma32(x1, wp[3], d3);
    pin4(d0, d1, d2, d3);
    if (lane < 16) *(LAS f32x4*)(L + ol) = f32x4{d0.x, d1.x, d2.x, d3.x};
    LGKM0();
    __builtin_amdgcn_s_barrier();
    CBAR();
    const float t = rbf(sum8((const LAS float*)(L + or_)));
    const float o = dppf<ROR8>(t);
    u32x4 act = spread8(bfbits(rbf(silu((a8 & 1) ? o : t)) * ((a8 & 1) ? t : o)), lane);
    f32x4 d[4];
#pragma unroll
    for (int j = 0; j < 4; ++j) d[j] = mfma32(act, fd[j], z);
    pin4(d[0], d[1], d[2], d[3]);
    if (lane < 16) WS_ST4(canon4(d[0].x, d[1].x, d[2].x, d[3].x), os);
  } else if (role == 0) {
    __builtin_amdgcn_s_barrier();
  }
}

extern "C" void kernel_launch(void* const* d_in, const int* in_sizes, int n_in, void* d_out, int out_size, void* d_ws,
                              size_t ws_size, hipStream_t stream) {
  decode_block<<<NWG, NTHR, 0, stream>>>((const u16*)d_in[0], (const u16*)d_in[1], (const u16*)d_in[2], (const u16*)d_in[3],
                                         (const u16*)d_in[4], (const u16*)d_in[6], (const u16*)d_in[7], (const u16*)d_in[8],
                                         (const u16*)d_in[9], (const u16*)d_in[10], (u16*)d_out, (unsigned char*)d_ws);
}
```

```cpp
#include <hip/hip_runtime.h>
#include <hip/hip_bf16.h>
#include <stdint.h>

typedef unsigned short u16;
typedef unsigned u32;
typedef unsigned long long u64;
typedef u32 u32x2 __attribute__((ext_vector_type(2)));
typedef u32 u32x4 __attribute__((ext_vector_type(4)));
typedef float f32x2 __attribute__((ext_vector_type(2)));
typedef float f32x4 __attribute__((ext_vector_type(4)));
typedef short s16x4 __attribute__((ext_vector_type(4)));
typedef __bf16 bf16x2 __attribute__((ext_vector_type(2)));
typedef __bf16 bf16x8 __attribute__((ext_vector_type(8)));
#define GAS __attribute__((address_space(1)))
#define LAS __attribute__((address_space(3)))
#define RLX_AGENT __ATOMIC_RELAXED, __HIP_MEMORY_SCOPE_AGENT
#define RLX_WG __ATOMIC_RELAXED, __HIP_MEMORY_SCOPE_WORKGROUP

constexpr int D = 512, DH = 128, SK = 65536, FF = 1792, NQ = 4096;
constexpr int NWG = 256, NTHR = 640, NCW = 8;
constexpr int CHUNK = (SK / 32) * DH * 2;
constexpr int NTILE = CHUNK / 4096;
constexpr int NIT = NTILE / NCW;
constexpr int NF = 56;
constexpr int G = 2;
constexpr int NS = 34;
constexpr int WPUB = 24;
constexpr int WMAX = 56;
constexpr int PRE = 6;
constexpr int QAT = 2;
constexpr int ND = 4;
constexpr int T_START = 190;
constexpr int T_PACE = 3540;
constexpr int SLOW_N = 40, T_SLOW = 52;
constexpr int T_LEAD = 35;
constexpr int T_CATCH = 15;
constexpr int T_T0 = 250, T_H = 670, T_Q = 1120;
constexpr int T_ML = 600;
constexpr int RETRY = 16;
constexpr int T_H1 = 145, T_A = 170, T_B = T_A + 95, T_C = T_B + 73, T_D = T_C + 137;
constexpr int T_AGAIN = 22;
constexpr int CODE = 676;
static_assert(NS % G == 0 && WPUB % 4 == 0 && WMAX % 4 == 0 && WMAX <= 60 && ND % G == 0 && QAT >= 1 && QAT <= PRE, "geometry");
constexpr u32 TSTEP = (u32)(((u64)T_PACE << 16) / (2 * NTILE / G)), TSLOW = (u32)(((u64)T_SLOW << 16) / (SLOW_N / G));
constexpr u32 t_group(int n) { return T_START + T_SLOW + (u32)(((u64)TSTEP * n) >> 16); }
constexpr u32 T_LAST = t_group(2 * NTILE / G - 1);
constexpr int SKEW0 = 512;
__device__ constexpr int stream_shift(int skew) { return skew > T_LEAD ? skew - T_LEAD : skew < 0 ? skew : 0; }
constexpr u32 SPIN_LDS = 1u << 19, SPIN_MEM = 1u << 17;
constexpr u32 WS_EPOCH = 0, WS_LATE = 1024, WS_T0 = 2048, WS_READY = 4096, WS_H = 8192, WS_Q = WS_H + 8192, WS_ML = WS_Q + 8 * 4096,
              WS_MOD = WS_ML + 8 * 1024, WS_O = WS_MOD + 4096, WS_M2 = WS_O + 64 * 8192, WS_M4 = WS_M2 + 64 * 2048,
              WS_X1 = WS_M4 + 64 * 2048, WS_END = WS_X1 + 64 * 1024;
constexpr int L_CTL = 0, L_OPK = 128, L_FF = L_OPK + 128, L_ML = 512, L_MODV = L_ML + 256, L_QB = 1024, L_RED = L_QB + 1024,
              L_SC = L_RED + 2048, L_RING = L_SC + NCW * 2048, L_END = L_RING + NS * 4096;
static_assert(L_END <= 163840, "LDS budget");
enum { C_DONE = 0, C_LANDED = 8, C_CNT1 = 9, C_MRG = 10, C_QRDY = 11, C_MLCNT = 12, C_FRDY = 13, C_MODRDY = 14, C_SKEW = 16, C_N = 17, C_BASE = 17 };

constexpr float LOG2E = 1.4426950408889634f, SCALE = 0.08838834764831845f;

#define CBAR() asm volatile("" ::: "memory")
#define SBAR() __builtin_amdgcn_sched_barrier(0)
#define LGKM0() asm volatile("s_waitcnt lgkmcnt(0)" ::: "memory")
#define VM0() asm volatile("s_waitcnt vmcnt(0)" ::: "memory")
#define SPIN(n, cap) _Pragma("unroll 1") for (u32 n = 0; n < (cap); ++n)
__device__ __forceinline__ float asf(u32 x) { return __builtin_bit_cast(float, x); }
__device__ __forceinline__ u32 asu(float x) { return __builtin_bit_cast(u32, x); }
__device__ __forceinline__ u32 pk2(float lo, float hi) { f32x2 t = {lo, hi}; return __builtin_bit_cast(u32, __builtin_convertvector(t, bf16x2)); }
__device__ __forceinline__ float lo16(u32 x) { return asf(x << 16); }
__device__ __forceinline__ float hi16(u32 x) { return asf(x & 0xffff0000u); }
__device__ __forceinline__ float rbf(float x) { return lo16(pk2(x, x)); }
__device__ __forceinline__ u32 bfbits(float x) { return pk2(x, x) & 0xffffu; }
__device__ __forceinline__ u32 canon(u32 x) { return x < 0xfffffffeu ? x : 0xfffffffeu; }
__device__ __forceinline__ u32x4 canon4(float a, float b, float c, float d) { return u32x4{canon(asu(a)), canon(asu(b)), canon(asu(c)), canon(asu(d))}; }
__device__ __forceinline__ float ex2(float x) { return __builtin_amdgcn_exp2f(x); }
__device__ __forceinline__ float rcp(float x) { return __builtin_amdgcn_rcpf(x); }
__device__ __forceinline__ float silu(float x) { return x * rcp(1.f + ex2(-LOG2E * x)); }
__device__ __forceinline__ float silu_acc(float x) { return x / (1.f + expf(-x)); }
__device__ __forceinline__ u32 now32() { return (u32)__builtin_amdgcn_s_memrealtime(); }
__device__ __forceinline__ void until(u32 t) { while ((u32)(t - now32() - 1) < 8192u) ; }
template <int CTRL> __device__ __forceinline__ u32 dppu(u32 x) { return (u32)__builtin_amdgcn_mov_dpp((int)x, CTRL, 0xf, 0xf, true); }
template <int CTRL> __device__ __forceinline__ float dppf(float x) { return asf(dppu<CTRL>(asu(x))); }
constexpr int XOR1 = 0xB1, XOR2 = 0x4E, QB0 = 0x00, QB2 = 0xAA, HMIR = 0x141, ROR4 = 0x124, ROR8 = 0x128;
__device__ __forceinline__ float sum32(float a, float b) { auto r = __builtin_amdgcn_permlane32_swap(asu(a), asu(b), false, false); return asf(r[0]) + asf(r[1]); }
__device__ __forceinline__ float sum16(float a, float b) { auto r = __builtin_amdgcn_permlane16_swap(asu(a), asu(b), false, false); return asf(r[0]) + asf(r[1]); }
__device__ __forceinline__ float max32(float a) { auto r = __builtin_amdgcn_permlane32_swap(asu(a), asu(a), false, false); return fmaxf(asf(r[0]), asf(r[1])); }
__device__ __forceinline__ float max16(float a) { auto r = __builtin_amdgcn_permlane16_swap(asu(a), asu(a), false, false); return fmaxf(asf(r[0]), asf(r[1])); }
__device__ __forceinline__ float dot2(u32 w, u32 x, float acc) { return __builtin_amdgcn_fdot2_f32_bf16(__builtin_bit_cast(bf16x2, w), __builtin_bit_cast(bf16x2, x), acc, false); }
__device__ __forceinline__ f32x4 mfma32(u32x4 a, u32x4 b, f32x4 c) { return __builtin_amdgcn_mfma_f32_16x16x32_bf16(__builtin_bit_cast(bf16x8, a), __builtin_bit_cast(bf16x8, b), c, 0, 0, 0); }
__device__ __forceinline__ f32x4 mfma16(u32x2 a, u32x2 b, f32x4 c) { return __builtin_amdgcn_mfma_f32_16x16x16bf16_1k(__builtin_bit_cast(s16x4, a), __builtin_bit_cast(s16x4, b), c, 0, 0, 0); }
__device__ __forceinline__ u32 rfl(u32 x) { return (u32)__builtin_amdgcn_readfirstlane((int)x); }
template <class T> __device__ __forceinline__ T ldnt(const void* p) { return __builtin_nontemporal_load((const GAS T*)p); }
__device__ __forceinline__ bool bad4(u32x4 v) { return max(max(v.x, v.y), max(v.z, v.w)) == 0xffffffffu; }
__device__ __forceinline__ u32 plo(u32 r1, u32 r0) { return __builtin_amdgcn_perm(r1, r0, 0x05040100u); }
__device__ __forceinline__ u32 phi(u32 r1, u32 r0) { return __builtin_amdgcn_perm(r1, r0, 0x07060302u); }
__device__ __forceinline__ float sum8(const LAS float* p) {
  return ((p[0] + p[64]) + (p[128] + p[192])) + ((p[256] + p[320]) + (p[384] + p[448]));
}
__device__ __forceinline__ void pin(u32& v) { asm volatile("" : "+v"(v)); }
__device__ __forceinline__ void pin4(f32x4& a, f32x4& b, f32x4& c, f32x4& d) { asm volatile("" : "+v"(a), "+v"(b), "+v"(c), "+v"(d)); }
__device__ __forceinline__ void pin(u32x4& v) { asm volatile("" : "+v"(v)); }

__device__ __forceinline__ float red4_parity(float v0, float v1, float v2, float v3) {
  float t = sum16(sum32(v0, v1), sum32(v2, v3));
  t += dppf<XOR2>(t); t += dppf<ROR4>(t); t += dppf<ROR8>(t);
  return t;
}
__device__ __forceinline__ float sum_parity(float t) { t += dppf<XOR2>(t); t += dppf<ROR4>(t); t += dppf<ROR8>(t); t = sum16(t, t); return sum32(t, t); }
__device__ __forceinline__ float max_parity(float t) { t = fmaxf(t, dppf<XOR2>(t)); t = fmaxf(t, dppf<ROR4>(t)); t = fmaxf(t, dppf<ROR8>(t)); return max32(max16(t)); }
__device__ __forceinline__ void ln_stats(u32x4 v, float& mean, float& vare) {
  const float piv = lo16(rfl(v.x));
  f32x2 d[4];
#pragma unroll
  for (int i = 0; i < 4; ++i) d[i] = f32x2{lo16(v[i]), hi16(v[i])} - piv;
  const f32x2 s2 = (d[0] + d[1]) + (d[2] + d[3]), q2 = __builtin_elementwise_fma(d[0], d[0], d[1] * d[1]) + __builtin_elementwise_fma(d[2], d[2], d[3] * d[3]);
  float t = sum32(s2.x + s2.y, q2.x + q2.y);
  t = sum16(t, t);
  t += dppf<XOR1>(t); t += dppf<XOR2>(t); t += dppf<ROR4>(t); t += dppf<ROR8>(t);
  auto r = __builtin_amdgcn_permlane32_swap(asu(t), asu(t), false, false);
  const float ds = asf(r[0]) * (1.f / D), dq = asf(r[1]) * (1.f / D);
  mean = rbf(piv + ds);
  const float var = rbf(fmaxf(fmaf(-ds, ds, dq), 0.f));
  vare = rbf(var + 1e-5f);
}
template <int NR> __device__ __forceinline__ void pair_rows(const u32x4 (&w)[NR], u32x4 (&p)[NR]) {
#pragma unroll
  for (int i = 0; i < NR; i += 2) {
    p[i] = u32x4{plo(w[i + 1].x, w[i].x), phi(w[i + 1].x, w[i].x), plo(w[i + 1].y, w[i].y), phi(w[i + 1].y, w[i].y)};
    p[i + 1] = u32x4{plo(w[i + 1].z, w[i].z), phi(w[i + 1].z, w[i].z), plo(w[i + 1].w, w[i].w), phi(w[i + 1].w, w[i].w)};
    pin(p[i]); pin(p[i + 1]);
  }
}
template <int NR> __device__ __forceinline__ void dots(const u32x4 (&p)[NR], const u32 (&x)[NR / 2], float& w0, float& w1) {
  float v[8];
#pragma unroll
  for (int j = 0; j < 8; ++j) { v[j] = 0.f;
#pragma unroll
    for (int i = 0; i < NR / 2; ++i) v[j] = dot2(p[2 * i + (j >> 2)][j & 3], x[i], v[j]); }
  w0 = sum16(sum32(v[0], v[1]), sum32(v[2], v[3])); w1 = sum16(sum32(v[4], v[5]), sum32(v[6], v[7]));
  w0 += dppf<ROR8>(w0); w1 += dppf<ROR8>(w1);
}
__device__ __forceinline__ float gemv_wide(const u32x4 (&p)[8], const u32 (&x)[4], int lane) { float a, b; dots<8>(p, x, a, b); return (lane & 8) ? b : a; }
__device__ __forceinline__ float gemv_narrow(const u32x4 (&p)[4], const u32 (&x)[2], int lane) {
  float a, b; dots<4>(p, x, a, b); a += dppf<ROR4>(a); b += dppf<ROR4>(b); return (lane & 8) ? b : a;
}
__device__ __forceinline__ u32x4 spread8(u32 v, int lane) {
  const u32 pk = __builtin_amdgcn_perm(dppu<XOR1>(v), v, (lane & 1) ? 0x01000504u : 0x05040100u);
  const u32 a = dppu<QB0>(pk), b = dppu<QB2>(pk);
  return u32x4{a, b, dppu<HMIR>(a), dppu<HMIR>(b)};
}
__device__ __forceinline__ void spread4(u32 v, int lane, u32 (&x)[2]) {
  const u32 pk = __builtin_amdgcn_perm(dppu<XOR1>(v), v, (lane & 1) ? 0x01000504u : 0x05040100u);
  x[0] = dppu<QB0>(pk); x[1] = dppu<QB2>(pk);
}
__device__ __forceinline__ void frag2(u32x4 a, u32x4 b, u32x4& c0, u32x4& c1) {
  c0 = u32x4{plo(a.y, a.x), plo(a.w, a.z), plo(b.y, b.x), plo(b.w, b.z)};
  c1 = u32x4{phi(a.y, a.x), phi(a.w, a.z), phi(b.y, b.x), phi(b.w, b.z)};
  pin(c0); pin(c1);
}
__device__ __forceinline__ void frag4(u32x4 a, u32x4 b, u32x4 c, u32x4 d, u32x4 (&f)[4]) {
  frag2(u32x4{a.x, a.z, b.x, b.z}, u32x4{c.x, c.z, d.x, d.z}, f[0], f[1]);
  frag2(u32x4{a.y, a.w, b.y, b.w}, u32x4{c.y, c.w, d.y, d.w}, f[2], f[3]);
}

__global__ void __launch_bounds__(NTHR, 3)
decode_block(const u16* __restrict__ xin, const u16* __restrict__ kc, const u16* __restrict__ vc, const u16* __restrict__ wq,
             const u16* __restrict__ wo, const u16* __restrict__ wg, const u16* __restrict__ wu, const u16* __restrict__ wd,
             const u16* __restrict__ cin, const u16* __restrict__ wmod, u16* __restrict__ out, unsigned char* ws) {
  u32 junk = 0;
  if (threadIdx.x < (CODE + 63) / 64) {
    u64 pc;
    asm volatile("s_getpc_b64 %1\n\tglobal_load_dword %0, %2, %1" : "+v"(junk), "=&s"(pc) : "v"((blockIdx.x >> 3) * CODE + threadIdx.x * 64));
  }
  __shared__ __attribute__((aligned(16))) unsigned char lds_raw[L_END];
  LAS unsigned char* L = (LAS unsigned char*)lds_raw;
  volatile LAS u32* ctl = (volatile LAS u32*)(L + L_CTL);
  const int lane = threadIdx.x & 63, wave = (int)rfl(threadIdx.x >> 6);
  const int b = blockIdx.x, x = b & 7, s = b >> 3, role = s >> 3, ri = s & 7;
  const int rid = 8 * x + ri;
  const int fid = 14 * (x >> 1) + 8 * (x & 1) + ri;
  const int mtask = 4 * x + (ri & 3);
  const bool isF = role == 3 && ri < 8 - 2 * (x & 1);
  const u64 kv_off = (u64)(x * 32 + s) * CHUNK;
#define wsr __builtin_amdgcn_make_buffer_rsrc(ws, 0, WS_END, 0x00020000)
#define WS_LD4(off) __builtin_amdgcn_raw_buffer_load_b128(wsr, (off), 0, 16)
#define WS_LD1(off) __builtin_amdgcn_raw_buffer_load_b32(wsr, (off), 0, 16)
#define WS_ST4(v, off) __builtin_amdgcn_raw_buffer_store_b128((v), wsr, (off), 0, 16)
#define WS_ST2(v, off) __builtin_amdgcn_raw_buffer_store_b64((v), wsr, (off), 0, 16)
#define WS_ST1(v, off) __builtin_amdgcn_raw_buffer_store_b32((v), wsr, (off), 0, 16)
#define WS_G(off) ((GAS u64*)(ws + (off)))
  auto wait_lds = [&](int word, u32 need) {
    SPIN(n, SPIN_LDS) { if (rfl(ctl[word]) >= need) break; __builtin_amdgcn_s_sleep(1); }
    CBAR();
  };

  if (wave == 0) {
    const u32 t0 = now32();
    ctl[C_BASE] = t0;
    const u64 ka = (u64)kc + kv_off, va = (u64)vc + kv_off;
    const u32 lbase = (u32)(uintptr_t)L + L_RING;
    const int r = lane >> 4, pos = lane & 15;
    LGKM0();
    __builtin_amdgcn_s_barrier();
    __builtin_amdgcn_s_setprio(3);
    asm volatile("s_waitcnt vmcnt(0)" : "+v"(junk));
    u32 tnext = (u32)T_START << 16, slot = 0, pub = 0, now = 0, room = NS, known = 0;
    auto freed = [&]() {
      u32 d = ctl[C_DONE + (lane & 7)];
      d = min(d, dppu<XOR1>(d)); d = min(d, dppu<XOR2>(d)); d = min(d, dppu<HMIR>(d));
      return rfl(d);
    };
#pragma unroll 1
    for (int ph = 0; ph < 2; ++ph) {
      const u64 a = ph ? va : ka;
      const u32x4 rs = {(u32)a, (u32)(a >> 32) & 0xffffu, (u32)CHUNK, 0x00020000u};
      const int sw = ph ? 0 : 15;
      const u32 v0 = r * 256 + ((pos ^ (r & sw)) << 4), v1 = r * 256 + ((pos ^ ((4 + r) & sw)) << 4),
                v2 = r * 256 + ((pos ^ ((8 + r) & sw)) << 4), v3 = r * 256 + ((pos ^ ((12 + r) & sw)) << 4);
      const int tend = ph ? NTILE - ND : NTILE;
      int skip = ph ? NTILE - 8 * (ND - 1) - 1 : -1;
      u32 goff = 0;
#pragma unroll 1
      for (int t = 0; t < tend; t += G) {
        const u32 tile = ph * NTILE + t;
        if (!known && (known = rfl(ctl[C_SKEW]))) tnext += (u32)max(stream_shift((int)known - SKEW0), -(int)(tnext >> 16)) << 16;
        const bool late = now >= (tnext >> 16);
        while (now < (tnext >> 16)) now = now32() - t0;
        tnext += tile < SLOW_N ? TSTEP + TSLOW : TSTEP;
        if (tile + G > room) {
          room = freed() + NS;
          if (tile + G > room) {
            VM0(); pub = tile; ctl[C_LANDED] = pub;
            SPIN(n, SPIN_LDS) { __builtin_amdgcn_s_sleep(1); room = freed() + NS; if (tile + G <= room) break; }
          }
        }
        u32 lag;
        if (late) { asm volatile("s_waitcnt vmcnt(%0)" ::"n"(WMAX - 4 * G) : "memory"); lag = WMAX / 4 - G; }
        else { asm volatile("s_waitcnt vmcnt(%0)" ::"n"(WPUB) : "memory"); lag = WPUB / 4; }
        if (tile > pub + lag) { pub = tile - lag; ctl[C_LANDED] = pub; }
#pragma unroll
        for (int i = 0; i < G; ++i) {
          asm volatile("s_mov_b32 m0, %0\n\ts_nop 0\n\t"
                       "buffer_load_dwordx4 %1, %5, %6 offen nt lds\n\t"
                       "buffer_load_dwordx4 %2, %5, %6 offen offset:1024 nt lds\n\t"
                       "buffer_load_dwordx4 %3, %5, %6 offen offset:2048 nt lds\n\t"
                       "buffer_load_dwordx4 %4, %5, %6 offen offset:3072 nt lds"
                       ::"s"(lbase + (slot + i) * 4096), "v"(v0), "v"(v1), "v"(v2), "v"(v3), "s"(rs),
                         "s"(goff) : "memory");
          const bool jump = t + i + 1 == skip;
          goff = rfl(goff + (jump ? 8192 : 4096)); skip += jump ? 7 : 0;
        }
        slot += G; if (slot >= NS) slot = 0;
      }
    }
#pragma unroll
    for (int k = WMAX / 4 - 1; k >= 0; --k) {
      asm volatile("s_waitcnt vmcnt(%0)" ::"n"(4 * k) : "memory");
      if (2 * NTILE - ND - k > pub) ctl[C_LANDED] = 2 * NTILE - ND - k;
    }
    return;
  }

  if (wave == 1) {
    if (lane < C_N) ctl[lane] = lane < NCW ? lane : 0;
    LGKM0();
    __builtin_amdgcn_s_barrier();
    SBAR();
    const u32 t0 = rfl(ctl[C_BASE]);
    const u64 ep = __hip_atomic_load(WS_G(WS_EPOCH), RLX_AGENT);
    const int j8 = 4 * (lane & 1) + (lane >> 5) + 2 * ((lane >> 4) & 1);
    const u32 xraw = *(const GAS u16*)(xin + 8 * rid + j8);
    const u32x4 ones = {~0u, ~0u, ~0u, ~0u};
    if (b == 0 && lane == 0) WS_ST1(0u, WS_LATE);
    if (lane < 2) WS_ST4(ones, WS_ML + x * 1024 + s * 32 + lane * 16);
    if (role == 0) {
      const u32x4 tail = lane < 48 ? ones : u32x4{0u, 0u, 0u, 0u};
      if (ri >= 4 && lane < 8) WS_ST4(ones, WS_MOD + mtask * 128 + lane * 16);
      WS_ST4(ones, WS_M2 + rid * 2048 + lane * 16); WS_ST4(ones, WS_M2 + rid * 2048 + 1024 + lane * 16);
      WS_ST4(ones, WS_M4 + rid * 2048 + lane * 16); WS_ST4(tail, WS_M4 + rid * 2048 + 1024 + lane * 16);
    } else if (role == 2) {
#pragma unroll
      for (int i = 0; i < 8; ++i) WS_ST4(ones, WS_O + rid * 8192 + i * 1024 + lane * 16);
    } else if (isF) {
      WS_ST4(ones, WS_X1 + fid * 1024 + lane * 16);
    }
    SBAR();
    const u64 tag = ep + 1;
    if (s == 0 && lane == 0) __hip_atomic_store(WS_G(WS_T0 + x * 128), (tag << 32) | t0, RLX_AGENT);
    VM0();
    if (lane == 0) __hip_atomic_store(WS_G(WS_READY + b * 8), tag, RLX_AGENT);
    int skew;
    {
      int d;
      for (u32 t = T_T0;; t += 60) {
        until(t0 + t);
        const u64 e = __hip_atomic_load(WS_G(WS_T0 + (lane & 7) * 128), RLX_AGENT);
        const bool ok = (u32)(e >> 32) == (u32)tag;
        d = ok ? (int)((u32)e - t0) : -1000;
        if (__all(ok) || t > T_T0 + 200) break;
      }
      int e = d;
      d = max(d, (int)dppu<XOR1>(d)); d = max(d, (int)dppu<XOR2>(d)); d = max(d, (int)dppu<HMIR>(d));
      e = min(e, (int)dppu<XOR1>(e)); e = min(e, (int)dppu<XOR2>(e)); e = min(e, (int)dppu<HMIR>(e));
      skew = (int)rfl(d - min((d - e) >> 1, T_CATCH));
      skew = skew < -300 || skew > 300 ? 0 : skew;
      ctl[C_SKEW] = skew + SKEW0;
    }
    const u32 tail0 = t0 + skew + T_LAST;
    if (role == 0 && ri >= 4) {
      wait_lds(C_MODRDY, 1);
      const f32x2 mv = *(const LAS f32x2*)(L + L_MODV + (lane & 31) * 8);
      if (lane < 32) WS_ST1(canon(pk2(mv.x, mv.y)), WS_MOD + mtask * 128 + lane * 4);
    }
    {
      const u32 thi = (u32)(tag >> 16), tlo = (u32)tag & 0xffffu;
      u64 g[8];
      bool rdy;
      while (now32() - t0 < (u32)(T_Q + skew)) __builtin_amdgcn_s_sleep(2);
      SPIN(n, SPIN_MEM) {
        bool ok = true;
#pragma unroll
        for (int i = 0; i < 8; ++i) g[i] = __hip_atomic_load(WS_G(WS_Q + x * 4096 + i * 512 + lane * 8), RLX_AGENT);
        rdy = true;
#pragma unroll
        for (int i = 0; i < 4; ++i) rdy &= __hip_atomic_load(WS_G(WS_READY + (i * 64 + lane) * 8), RLX_AGENT) == tag;
#pragma unroll
        for (int i = 0; i < 8; ++i) ok &= (u32)(g[i] >> 32) == thi && ((u32)g[i] >> 16) == tlo;
        if (__all(ok && rdy)) break;
        __builtin_amdgcn_s_sleep(RETRY);
      }
#pragma unroll
      for (int i = 0; i < 8; ++i) *(LAS u16*)(L + L_QB + i * 128 + lane * 2) = (u16)g[i];
      CBAR();
      ctl[C_QRDY] = 1;
    }
    float ga, gb;
    {
      SPIN(n, SPIN_LDS) { if (rfl(ctl[C_MLCNT]) >= NCW) break; __builtin_amdgcn_s_sleep(8); }
      CBAR();
      const f32x2 ml = *(const LAS f32x2*)(L + L_ML + (lane & 31) * 8);
      float m = fmaxf(ml.x, dppf<ROR4>(ml.x)); m = fmaxf(m, dppf<ROR8>(m)); m = max16(m);
      float l = ml.y * ex2((ml.x - m) * LOG2E);
      l += dppf<ROR4>(l); l += dppf<ROR8>(l); l = sum16(l, l);
      const u32x2 v = {canon(asu(m)), canon(asu(l))};
      if (lane < 4) WS_ST2(v, WS_ML + x * 1024 + s * 32 + lane * 8);
      const u32 t1 = now32();
      while (now32() - t1 < T_ML) __builtin_amdgcn_s_sleep(16);
      u32x4 g;
      u32 g0 = 0, g1 = 0;
      SPIN(n, SPIN_MEM) {
        CBAR();
        g = WS_LD4(WS_ML + x * 1024 + lane * 16);
        if (role == 0) { g0 = WS_LD1(WS_MOD + rid * 16 + (j8 >> 1) * 4); g1 = WS_LD1(WS_MOD + 3072 + rid * 16 + (j8 >> 1) * 4); }
        if (!__any(bad4(g) || max(g0, g1) == 0xffffffffu)) break;
        __builtin_amdgcn_s_sleep(RETRY);
      }
      ga = (j8 & 1) ? hi16(g0) : lo16(g0); gb = (j8 & 1) ? hi16(g1) : lo16(g1);
      const float ma = max_parity(asf(g.x)), mb = max_parity(asf(g.z));
      const float la = sum_parity(asf(g.y) * ex2((asf(g.x) - ma) * LOG2E)), lb = sum_parity(asf(g.w) * ex2((asf(g.z) - mb) * LOG2E));
      if (lane < 2) *(LAS f32x4*)(L + L_FF + lane * 16) = f32x4{ma, 1.f / la, mb, 1.f / lb};
      CBAR();
      ctl[C_FRDY] = 1;
    }
    if (role != 0) return;
    LGKM0();
    __builtin_amdgcn_s_barrier();
    auto gather64 = [&](u32 off, u32 t) {
      u32x4 g0, g1;
      SPIN(n, SPIN_MEM) {
        until(t); t += T_AGAIN;
        CBAR();
        g0 = WS_LD4(off); g1 = WS_LD4(off + 1024);
        if (!__any(bad4(g0) || bad4(g1))) break;
      }
      return red4_parity(asf(g0.x) + asf(g1.x), asf(g0.y) + asf(g1.y), asf(g0.z) + asf(g1.z), asf(g0.w) + asf(g1.w));
    };
    u32 o2 = WS_M2 + rid * 2048 + lane * 16, o4 = WS_M4 + rid * 2048 + lane * 16, ox = WS_X1 + lane * 1024 + rid * 16, oo = (8 * rid + j8) * 2;
    pin(o2); pin(o4); pin(ox); pin(oo);
    float xres = lo16(xraw);
    asm volatile("" : "+v"(ga), "+v"(gb), "+v"(xres));
    until(tail0 + T_B - 40);
    CBAR();
    const u32 late = min(rfl(WS_LD1(WS_LATE)), 400u);
    const float att = gather64(o2, tail0 + late + T_B);
    const float x1 = rbf(xres + rbf(ga * rbf(att)));
    {
      auto t = __builtin_amdgcn_permlane32_swap(asu(x1), asu(x1), false, false);
      const u32 pk = canon(phi(t[1], t[0]));
      auto u = __builtin_amdgcn_permlane16_swap(pk, pk, false, false);
      const u32 n0 = dppu<XOR1>(u[0]), n1 = dppu<XOR1>(u[1]);
      const u32x4 v = (lane & 1) ? u32x4{n0, n1, u[0], u[1]} : u32x4{u[0], u[1], n0, n1};
      if (lane < NF) WS_ST4(v, ox);
    }
    if (b == 0 && lane == 0) __hip_atomic_store(WS_G(WS_EPOCH), tag, RLX_AGENT);
    const float y = gather64(o4, tail0 + late + T_D);
    if ((lane & 14) == 0) *(GAS u16*)((GAS unsigned char*)out + oo) = (u16)bfbits(x1 + rbf(gb * rbf(y)));
    return;
  }

  const int w = wave - 2, g = lane >> 4, i16 = lane & 15, a8 = lane >> 3, ch = lane & 7, b2 = (lane >> 2) & 1;
  const int jcol = (lane >> 5) + 2 * ((lane >> 4) & 1) + 4 * ((lane >> 3) & 1);
  LAS float* red = (LAS float*)(L + L_RED);
  auto last_arriver = [&]() {
    CBAR();
    u32 old = 0;
    if (lane == 0) old = __hip_atomic_fetch_add((LAS u32*)(L + L_CTL) + C_CNT1, 1u, RLX_WG);
    const bool last = rfl(old) == NCW - 1;
    CBAR();
    return last;
  };
  u32x4 wp[8];
  u32x4 wu0[2];
  u64 tag = 0;
  float lnx = 0.f;
  const int r4 = 64 * w + 4 * (lane >> 2), qtask = 16 * x + (role == 3 ? 8 : 0) + ri;
  if (role == 2) {
    SBAR();
    __builtin_amdgcn_s_barrier();
    SBAR();
#pragma unroll
    for (int i = 0; i < 8; ++i) {
      const u16* p = wo + (size_t)(64 * rid + 32 * (i >> 2) + 8 * g + 2 * (i & 3)) * D + 64 * w + 4 * i16;
      const u32x2 r0 = ldnt<u32x2>(p), r1 = ldnt<u32x2>(p + D);
      wp[i] = u32x4{r0.x, r0.y, r1.x, r1.y};
    }
  } else if (role == 0 && ri >= 4) {
    u32x4 wn[8];
    SBAR();
    __builtin_amdgcn_s_barrier();
    SBAR();
#pragma unroll
    for (int i = 0; i < 8; ++i) wn[i] = ldnt<u32x4>(wmod + (size_t)(64 * w + 8 * a8 + (i ^ (4 * b2))) * (6 * D) + 2 * D + 64 * mtask + 8 * ch);
    SBAR();
    const u32 craw = *(const GAS u16*)(cin + 64 * w + lane);
    SBAR();
    const u32x4 sv = spread8(bfbits(silu_acc(lo16(craw))), lane);
    const u32 xs[4] = {sv.x, sv.y, sv.z, sv.w};
    pair_rows<8>(wn, wp);
    red[w * 64 + 8 * ch + jcol] = gemv_wide(wp, xs, lane);
    if (last_arriver()) {
      float t = sum8(red + lane);
      t = rbf(t);
      if ((mtask >> 3) == 2) t = rbf(1.f + t);
      ((LAS float*)(L + L_MODV))[lane] = t;
      CBAR();
      ctl[C_MODRDY] = 1;
    }
  } else if (role != 0) {
    u32x4 wn[4];
    const u16* p0 = wq + (size_t)r4 * NQ + 32 * qtask + 8 * (lane & 3);
    wn[0] = ldnt<u32x4>(p0); wn[1] = ldnt<u32x4>(p0 + NQ);
    SBAR();
    __builtin_amdgcn_s_barrier();
    SBAR();
    wn[2] = ldnt<u32x4>(p0 + 2 * NQ); wn[3] = ldnt<u32x4>(p0 + 3 * NQ);
    SBAR();
    const u32x4 xv = *(const GAS u32x4*)(xin + lane * 8);
    const u32 xraw = *(const GAS u16*)(xin + 64 * w + lane);
    const u64 ep = __hip_atomic_load(WS_G(WS_EPOCH), RLX_AGENT);
    SBAR();
    if (isF) {
      const u16* p = wg + (size_t)(64 * w + 8 * g) * FF + 32 * fid + 2 * i16;
#pragma unroll
      for (int i = 0; i < 4; ++i) {
        const u16* q = p + (32 * (i >> 1) + 4 * (i & 1)) * FF;
        wp[4 + i] = u32x4{ldnt<u32>(q), ldnt<u32>(q + FF), ldnt<u32>(q + 2 * FF), ldnt<u32>(q + 3 * FF)};
      }
#pragma unroll
      for (int i = 0; i < 2; ++i) {
        const u16* q = wu + (p - wg) + 4 * i * FF;
        wu0[i] = u32x4{ldnt<u32>(q), ldnt<u32>(q + FF), ldnt<u32>(q + 2 * FF), ldnt<u32>(q + 3 * FF)};
      }
    }
    SBAR();
    float mean, vare;
    ln_stats(xv, mean, vare);
    lnx = rbf(rbf(lo16(xraw) - mean) / rbf(sqrtf(vare)));
    asm volatile("" : "+v"(lnx));
    tag = (((u64)rfl((u32)(ep >> 32)) << 32) | rfl((u32)ep)) + 1;
    pair_rows<4>(wn, (u32x4 (&)[4])wp[0]);
  } else {
    u32x4 wn[4], wp2[4];
    const u16* p = wmod + (size_t)r4 * (6 * D) + 32 * mtask + 8 * (lane & 3);
    wn[0] = ldnt<u32x4>(p); wn[1] = ldnt<u32x4>(p + 6 * D);
    SBAR();
    __builtin_amdgcn_s_barrier();
    SBAR();
    wn[2] = ldnt<u32x4>(p + 12 * D); wn[3] = ldnt<u32x4>(p + 18 * D);
    SBAR();
    const u32 craw = *(const GAS u16*)(cin + 64 * w + lane);
    const u64 ep = __hip_atomic_load(WS_G(WS_EPOCH), RLX_AGENT);
    SBAR();
    u32 xs[2];
    spread4(bfbits(silu_acc(lo16(craw))), lane, xs);
    pair_rows<4>(wn, wp2);
    red[w * 64 + 8 * (lane & 3) + jcol] = gemv_narrow(wp2, xs, lane);
    if (last_arriver()) {
      float t = sum8(red + (lane & 31));
      t = rbf(t);
      if (mtask >= 16) t = rbf(1.f + t);
      if (lane < 32) __hip_atomic_store(WS_G(WS_H + (32 * mtask + lane) * 8), ((ep + 1) << 16) | (asu(t) >> 16), RLX_AGENT);
    }
  }
  const u32 tw0 = rfl(ctl[C_BASE]);
  auto q_task = [&]() {
    u64 sg, cg;
    wait_lds(C_SKEW, 1);
    const u32 th = T_H + rfl(ctl[C_SKEW]) - SKEW0;
    while (now32() - tw0 < th) __builtin_amdgcn_s_sleep(1);
    SPIN(n, SPIN_MEM) {
      sg = __hip_atomic_load(WS_G(WS_H + (64 * w + lane) * 8), RLX_AGENT);
      cg = __hip_atomic_load(WS_G(WS_H + 4096 + (64 * w + lane) * 8), RLX_AGENT);
      if (__all((sg >> 16) == (tag & 0xffffffffffffull) && (cg >> 16) == (tag & 0xffffffffffffull))) break;
      __builtin_amdgcn_s_sleep(RETRY / 2);
    }
    u32 xs[2];
    spread4(pk2(rbf(lnx * lo16((u32)cg)) + lo16((u32)sg), 0.f), lane, xs);
    red[w * 64 + 8 * (lane & 3) + jcol] = gemv_narrow((const u32x4 (&)[4])wp[0], xs, lane);
    if (last_arriver()) {
      float t = sum8(red + (lane & 31));
      if (lane < 32) __hip_atomic_store(WS_G(WS_Q + (32 * qtask + lane) * 8), (tag << 16) | bfbits(t), RLX_AGENT);
    }
  };

  const int head = i16 >> 2;
  LAS unsigned char* sc = L + L_SC + w * 2048;
  u32 slot = w;
  auto fetch = [&](u32 pos, u32 o0, u32 o1, u32 o2, u32 o3, u32x4 (&f)[4]) {
    wait_lds(C_LANDED, pos + 1);
    const LAS unsigned char* p = L + L_RING + slot * 4096;
    f[0] = *(const LAS u32x4*)(p + o0); f[1] = *(const LAS u32x4*)(p + o1); f[2] = *(const LAS u32x4*)(p + o2); f[3] = *(const LAS u32x4*)(p + o3);
    SBAR();
    asm volatile("s_waitcnt lgkmcnt(0)" : "+v"(f[0]), "+v"(f[1]), "+v"(f[2]), "+v"(f[3]) :: "memory");
    ctl[C_DONE + w] = pos + NCW;
    slot += NCW; if (slot >= NS) slot -= NS;
  };
  float m_w;
  {
    const u32 ko = i16 * 256 + ((g ^ i16) << 4);
    static_assert(QAT >= 1 && QAT <= PRE, "the Q task runs between two parked tiles");
    u32x4 pre[PRE][4];
#pragma unroll
    for (int i = 0; i < PRE; ++i) {
      fetch(i * NCW + w, ko, ko ^ 64, ko ^ 128, ko ^ 192, pre[i]);
      if (i == QAT - 1 && (role & 1)) { q_task(); wp[0] = wu0[0]; wp[1] = wu0[1]; pin(wp[0]); pin(wp[1]); }
    }
    wait_lds(C_QRDY, 1);
    u32x4 qf[4];
#pragma unroll
    for (int ks = 0; ks < 4; ++ks) qf[ks] = *(const LAS u32x4*)(L + L_QB + head * 256 + ks * 64 + g * 16);
    float mx = -INFINITY;
    auto score = [&](int i, const u32x4 (&kf)[4]) {
      f32x4 d = {0.f, 0.f, 0.f, 0.f};
#pragma unroll
      for (int ks = 0; ks < 4; ++ks) d = mfma32(kf[ks], qf[ks], d);
      mx = fmaxf(fmaxf(mx, fmaxf(d.x, d.y)), fmaxf(d.z, d.w));
      const u32 r0 = pk2(d.x, d.y), r1 = pk2(d.z, d.w);
      const u32x2 z = {pk2(lo16(r0) * SCALE, hi16(r0) * SCALE), pk2(lo16(r1) * SCALE, hi16(r1) * SCALE)};
      if ((lane & 3) == 0) *(LAS u32x2*)(sc + i * 128 + g * 32 + head * 8) = z;
    };
#pragma unroll
    for (int i = 0; i < PRE; ++i) score(i, pre[i]);
#pragma unroll 1
    for (int i = PRE; i < NIT; ++i) {
      u32x4 kf[4];
      fetch(i * NCW + w, ko, ko ^ 64, ko ^ 128, ko ^ 192, kf);
      score(i, kf);
    }
    m_w = rbf(rbf(max32(max16(mx))) * SCALE);
  }
  __builtin_amdgcn_s_waitcnt(0x0F70);
  {
    const u32 mu = asu(m_w);
    const float m0 = asf(__builtin_amdgcn_readlane(mu, 0)), m1 = asf(__builtin_amdgcn_readlane(mu, 4)),
                m2 = asf(__builtin_amdgcn_readlane(mu, 8)), m3 = asf(__builtin_amdgcn_readlane(mu, 12));
    const float ma = (lane & 1) ? m2 : m0, mb = (lane & 1) ? m3 : m1;
    float la = 0.f, lb = 0.f;
    CBAR();
#pragma unroll
    for (int k = 0; k < 2; ++k) {
      u32x4 v = *(const LAS u32x4*)(sc + k * 1024 + lane * 16);
#pragma unroll
      for (int d = 0; d < 4; ++d) {
        const float mm = d < 2 ? ma : mb;
        const float p0 = ex2((lo16(v[d]) - mm) * LOG2E), p1 = ex2((hi16(v[d]) - mm) * LOG2E);
        if (d < 2) la += p0 + p1; else lb += p0 + p1;
        v[d] = pk2(p0, p1);
      }
      *(LAS u32x4*)(sc + k * 1024 + lane * 16) = v;
    }
    la = sum_parity(la); lb = sum_parity(lb);
    if (lane < 2) *(LAS f32x4*)(L + L_ML + w * 32 + lane * 16) = f32x4{ma, la, mb, lb};
    CBAR();
    if (lane == 0) __hip_atomic_fetch_add((LAS u32*)(L + L_CTL) + C_MLCNT, 1u, RLX_WG);
    m_w = g == 0 ? m0 : g == 1 ? m1 : g == 2 ? m2 : m3;
  }
  f32x4 acc0 = {0.f, 0.f, 0.f, 0.f}, acc1 = {0.f, 0.f, 0.f, 0.f};
  const int js = lane & 3;
  const u32 sx0 = js == 0 ? 0x0c0c0100u : js == 1 ? 0x01000c0cu : 0x0c0c0c0cu, sx1 = js == 0 ? 0x0c0c0302u : js == 1 ? 0x03020c0cu : 0x0c0c0c0cu;
  const u32 sy0 = js == 2 ? 0x0c0c0100u : js == 3 ? 0x01000c0cu : 0x0c0c0c0cu, sy1 = js == 2 ? 0x0c0c0302u : js == 3 ? 0x03020c0cu : 0x0c0c0c0cu;
  const u32 vo = g * 1024 + i16 * 16;
  auto p_frags = [&](int i, u32x2 (&pa)[4]) {
    const u32x2 p = *(const LAS u32x2*)(sc + i * 128 + g * 32 + head * 8);
#pragma unroll
    for (int t = 0; t < 4; ++t) pa[t] = u32x2{__builtin_amdgcn_perm(0u, p[t >> 1], (t & 1) ? sx1 : sx0), __builtin_amdgcn_perm(0u, p[t >> 1], (t & 1) ? sy1 : sy0)};
  };
  auto pv = [&](const u32x2 (&pa)[4], const u32x4 (&vf)[4]) {
#pragma unroll
    for (int t = 0; t < 4; ++t) { acc0 = mfma16(pa[t], u32x2{vf[t].x, vf[t].y}, acc0); acc1 = mfma16(pa[t], u32x2{vf[t].z, vf[t].w}, acc1); }
  };
  auto vtile = [&](int i, u32 pos) {
    u32x2 pa[4]; u32x4 vf[4];
    p_frags(i, pa);
    fetch(pos, vo, vo + 256, vo + 512, vo + 768, vf);
    pv(pa, vf);
  };
#pragma unroll 1
  for (int i = 0; i < NIT - ND; ++i) vtile(i, NTILE + i * NCW + w);
  wait_lds(C_FRDY, 1);
  const f32x2 ff = *(const LAS f32x2*)(L + L_FF + g * 8);
  float fac = ex2((m_w - ff.x) * LOG2E) * ff.y;
  asm volatile("" : "+v"(fac));
  if (role == 2) {
    u32x4 f0[4], f1[4];
    frag4(wp[0], wp[1], wp[2], wp[3], f0); frag4(wp[4], wp[5], wp[6], wp[7], f1);
#pragma unroll
    for (int j = 0; j < 4; ++j) { wp[j] = f0[j]; wp[4 + j] = f1[j]; }
  } else if (isF) {
#pragma unroll
    for (int j = 4; j < 8; j += 2) { u32x4 c0, c1; frag2(wp[j], wp[j + 1], c0, c1); wp[j] = c0; wp[j + 1] = c1; }
    { u32x4 c0, c1; frag2(wp[0], wp[1], c0, c1); wp[0] = c0; wp[1] = c1; }
  }
  const int skew = (int)rfl(ctl[C_SKEW]) - SKEW0;
  const u32 tailw = tw0 + skew + T_LAST;
  const int frow = 64 * w + 32 * (a8 & 1) + 8 * g + ch;
  u32 c1v = 0, shv = 0;
  if (isF) {
    SPIN(n, SPIN_MEM) {
      CBAR();
      c1v = WS_LD1(WS_MOD + 2048 + (frow >> 1) * 4); shv = WS_LD1(WS_MOD + 1024 + (frow >> 1) * 4);
      if (!__any(max(c1v, shv) == 0xffffffffu)) break;
      __builtin_amdgcn_s_sleep(RETRY);
    }
  }
  if (w < NCW - 1) {
#pragma unroll 1
    for (int i = NIT - ND; i < NIT; ++i) {
      vtile(i, NTILE + i * NCW + w - (i - (NIT - ND)));
      slot = slot ? slot - 1 : NS - 1;
    }
    *(LAS f32x4*)(sc + g * 512 + i16 * 32) = acc0 * fac;
    *(LAS f32x4*)(sc + g * 512 + i16 * 32 + 16) = acc1 * fac;
    CBAR();
    if (lane == 0) __hip_atomic_fetch_add((LAS u32*)(L + L_CTL) + C_MRG, 1u, RLX_WG);
  } else {
    u32 off = WS_O + (8 * x + 2 * g + (i16 >> 3)) * 8192 + ch * 1024 + s * 32;
    pin(off);
    u32x2 pa[2][4];
    u32x4 va[ND][4];
    const unsigned char* vp = (const unsigned char*)vc + kv_off + vo;

#pragma unroll
    for (int k = 0; k < ND; ++k) {
      if (k % G == 0) until(tw0 + stream_shift(skew) + t_group((2 * NTILE - ND + k) / G));
#pragma unroll
      for (int t = 0; t < 4; ++t) va[k][t] = ldnt<u32x4>(vp + (NTILE - 1 - 8 * (ND - 1 - k)) * 4096 + t * 256);
      SBAR();
    }
#pragma unroll
    for (int k = 0; k < ND - 2; ++k) { p_frags(NIT - ND + k, pa[0]); pv(pa[0], va[k]); }
    p_frags(NIT - 2, pa[0]); p_frags(NIT - 1, pa[1]);
    asm volatile("" : "+v"(pa[0][0]), "+v"(pa[0][1]), "+v"(pa[0][2]), "+v"(pa[0][3]), "+v"(pa[1][0]), "+v"(pa[1][1]), "+v"(pa[1][2]), "+v"(pa[1][3]), "+v"(acc0), "+v"(acc1));
    SBAR();
    SPIN(n, SPIN_LDS) { if (rfl(ctl[C_MRG]) >= NCW - 1) break; }
    CBAR();
    f32x4 o0 = {0.f, 0.f, 0.f, 0.f}, o1 = {0.f, 0.f, 0.f, 0.f};
#pragma unroll
    for (int i = 0; i < NCW - 1; ++i) {
      o0 += *(const LAS f32x4*)(L + L_SC + i * 2048 + g * 512 + i16 * 32); o1 += *(const LAS f32x4*)(L + L_SC + i * 2048 + g * 512 + i16 * 32 + 16);
    }
    asm volatile("" : "+v"(o0), "+v"(o1));
    SBAR();
    pv(pa[0], va[ND - 2]); pv(pa[1], va[ND - 1]);
    o0 += acc0 * fac; o1 += acc1 * fac;
    WS_ST4(canon4(o0.x, o0.y, o0.z, o0.w), off); WS_ST4(canon4(o1.x, o1.y, o1.z, o1.w), off + 16);
    const int d = (int)(now32() - tailw) - T_H1;
    if (d > 0 && lane == 0) __hip_atomic_fetch_max((GAS u32*)(ws + WS_LATE), (u32)d + 25u, RLX_AGENT);
  }

  if (role == 2) {
    u32 oi = WS_O + rid * 8192 + w * 1024 + lane * 16, os = WS_M2 + (8 * w + (i16 >> 1)) * 2048 + rid * 32 + (lane & 1) * 16,
        ol = L_OPK + w * 16 + (4 * (lane & 1) + (lane >> 5) + 2 * ((lane >> 4) & 1)) * 2, or_ = L_OPK + g * 16;
    pin(oi); pin(os); pin(ol); pin(or_);
    __builtin_amdgcn_s_barrier();
    const u32 ta = tailw + T_A;
    u32x4 v;
    SPIN(n, SPIN_MEM) {
      until(ta + n * T_AGAIN);
      CBAR();
      v = WS_LD4(oi);
      if (!__any(bad4(v))) break;
    }
    const float o = red4_parity(asf(v.x), asf(v.y), asf(v.z), asf(v.w));
    if ((lane & 14) == 0) *(LAS u16*)(L + ol) = (u16)bfbits(o);
    LGKM0();
    __builtin_amdgcn_s_barrier();
    CBAR();
    const u32x4 x0 = *(const LAS u32x4*)(L + or_), x1 = *(const LAS u32x4*)(L + or_ + 64);
    f32x4 d[4];
#pragma unroll
    for (int j = 0; j < 4; ++j) d[j] = mfma32(x0, wp[j], f32x4{0.f, 0.f, 0.f, 0.f});
#pragma unroll
    for (int j = 0; j < 4; ++j) d[j] = mfma32(x1, wp[4 + j], d[j]);
    pin4(d[0], d[1], d[2], d[3]);
    if (lane < 16) WS_ST4(canon4(d[0].x, d[1].x, d[2].x, d[3].x), os);
  } else if (isF) {
#define DMA1 "s_mov_b32 m0, %3\n\ts_nop 0\n\tbuffer_load_dwordx4 %1, %0, %2 offen nt lds"
    const u32 lb = L_RING + w * 8192, m = rfl((u32)(uintptr_t)L + lb);
    const u64 ua = (u64)wu + 64 * fid, da = (u64)wd + (u64)fid * 32 * D * 2;
    const u32x4 ru = {(u32)ua, (u32)(ua >> 32) & 0xffffu, D * FF * 2 - 64u * fid, 0x00020000u}, rd = {(u32)da, (u32)(da >> 32) & 0xffffu, 32 * D * 2, 0x00020000u};
    const u32x4 rw = {(u32)(uintptr_t)ws, (u32)((uintptr_t)ws >> 32) & 0xffffu, WS_END, 0x00020000u};
    const u32 vu = (64 * w + 2 * (lane >> 2)) * (FF * 2) + (lane & 3) * 16, vd = a8 * (D * 2) + w * 128 + ch * 16;
    __builtin_amdgcn_s_barrier();
#pragma unroll
    for (int i = 1; i < 4; i += 2) asm volatile(DMA1 ::"s"(ru), "v"(vu), "s"((32 * (i & 1) + (i >> 1)) * FF * 2), "s"(m + i * 1024) : "memory");
#pragma unroll
    for (int i = 0; i < 4; ++i) asm volatile(DMA1 ::"s"(rd), "v"(vd), "s"(i * 8 * D * 2), "s"(m + 4096 + i * 1024) : "memory");
    u32 lraw;
    asm volatile("buffer_load_dword %0, off, %1, 0 offset:%2 sc1" : "=v"(lraw) : "s"(rw), "n"(WS_LATE) : "memory");
    const u32 sh16 = (ch & 1) ? 0u : 16u;
    float c1 = hi16(c1v << sh16), sh = hi16(shv << sh16);
    asm volatile("s_waitcnt vmcnt(5)" : "+v"(c1), "+v"(sh) :: "memory");
#pragma unroll
    for (int ks = 1; ks < 2; ++ks) {
      const LAS u32* e = (const LAS u32*)(L + lb + ks * 1024 + g * 256 + i16 * 4);
      frag2(u32x4{e[0], e[512], e[16], e[528]}, u32x4{e[32], e[544], e[48], e[560]}, wp[2 * ks], wp[2 * ks + 1]);
    }
    u32 oa = WS_X1 + fid * 1024 + lane * 16, ob = WS_X1 + fid * 1024 + (frow >> 3) * 16 + (ch >> 1) * 4;
    u32 os = WS_M4 + (8 * w + (i16 >> 1)) * 2048 + fid * 32 + (lane & 1) * 16, ol = L_RED + w * 256 + lane * 16,
        or_ = L_RED + (16 * g + 4 * (ch >> 1) + (ch & 1) + 2 * (a8 & 1)) * 4;
    pin(oa); pin(ob); pin(os); pin(ol); pin(or_);
    asm volatile("s_waitcnt vmcnt(0)" : "+v"(lraw) :: "memory");
    auto col4 = [&](u32 o) { const LAS u32x2* p = (const LAS u32x2*)(L + o + g * 1024 + i16 * 8); const u32x2 r0 = p[0], r1 = p[16]; return u32x4{r0.x, r0.y, r1.x, r1.y}; };
    u32x4 fd[4];
    frag4(col4(lb + 4096), col4(lb + 4352), col4(lb + 4608), col4(lb + 4864), fd);
    const u32 late = min(rfl(lraw), 400u);
    u32x4 xa; u32 xb;
    SPIN(n, SPIN_MEM) {
      until(tailw + late + T_C + n * T_AGAIN);
      CBAR();
      xa = WS_LD4(oa); xb = WS_LD1(ob);
      if (!__any(bad4(xa) || xb == 0xffffffffu)) break;
    }
    float mean, vare;
    ln_stats(xa, mean, vare);
    const float rden = rcp(rbf(__builtin_amdgcn_sqrtf(vare)));
    const u32x4 x0 = spread8(pk2(rbf(rbf(rbf(hi16(xb << sh16) - mean) * rden) * c1) + sh, 0.f), lane);
    const u32x4 x1 = {dppu<ROR8>(x0.x), dppu<ROR8>(x0.y), dppu<ROR8>(x0.z), dppu<ROR8>(x0.w)};
    const f32x4 z = {0.f, 0.f, 0.f, 0.f};
    f32x4 d0 = mfma32(x0, wp[4], z), d1 = mfma32(x0, wp[5], z), d2 = mfma32(x0, wp[0], z), d3 = mfma32(x0, wp[1], z);
    d0 = mfma32(x1, wp[6], d0); d1 = mfma32(x1, wp[7], d1); d2 = mfma32(x1, wp[2], d2); d3 = mfma32(x1, wp[3], d3);
    pin4(d0, d1, d2, d3);
    if (lane < 16) *(LAS f32x4*)(L + ol) = f32x4{d0.x, d1.x, d2.x, d3.x};
    LGKM0();
    __builtin_amdgcn_s_barrier();
    CBAR();
    const float t = rbf(sum8((const LAS float*)(L + or_)));
    const float o = dppf<ROR8>(t);
    u32x4 act = spread8(bfbits(rbf(silu((a8 & 1) ? o : t)) * ((a8 & 1) ? t : o)), lane);
    f32x4 d[4];
#pragma unroll
    for (int j = 0; j < 4; ++j) d[j] = mfma32(act, fd[j], z);
    pin4(d[0], d[1], d[2], d[3]);
    if (lane < 16) WS_ST4(canon4(d[0].x, d[1].x, d[2].x, d[3].x), os);
  } else if (role == 0) {
    __builtin_amdgcn_s_barrier();
  }
}

extern "C" void kernel_launch(void* const* d_in, const int* in_sizes, int n_in, void* d_out, int out_size, void* d_ws,
                              size_t ws_size, hipStream_t stream) {
  decode_block<<<NWG, NTHR, 0, stream>>>((const u16*)d_in[0], (const u16*)d_in[1], (const u16*)d_in[2], (const u16*)d_in[3],
                                         (const u16*)d_in[4], (const u16*)d_in[6], (const u16*)d_in[7], (const u16*)d_in[8],
                                         (const u16*)d_in[9], (const u16*)d_in[10], (u16*)d_out, (unsigned char*)d_ws);
}
```
